```python
import torch

def _softmax(x, axis=-1):
    return torch.softmax(x, dim=axis)

def _gelu(x):
    return torch.nn.functional.gelu(x, approximate='tanh')

def _silu(x):
    return torch.nn.functional.silu(x)

def _sigmoid(x):
    return torch.sigmoid(x)

def _layernorm(x, axis=-1, eps=1e-05):
    m = x.mean(dim=axis, keepdim=True)
    v = x.var(dim=axis, keepdim=True, unbiased=False)
    return (x - m) / torch.sqrt(v + eps)

def _scatter_add(tgt, idx, src, axis=0):
    return tgt.clone().index_add_(axis, idx, src)

def _segment_sum(src, seg, n):
    out = torch.zeros((n,) + src.shape[1:], dtype=src.dtype)
    return out.index_add_(0, seg, src)

def _pad(x, pw):
    flat = tuple((v for pair in reversed(pw) for v in pair))
    return torch.nn.functional.pad(x, flat)
kernel_name = 'adaLN_cond__grouped_kv_mqa__glu_gated_reglu__prompt'

def setup_inputs():

    _w = lambda a: (lambda t: t.to(torch.bfloat16) if t.is_floating_point() else t)(torch.as_tensor(a))

    def _A_setup():
        B, S, D, H = (1, 2048, 576, 32)
        Dh = D // H
        return {'x': _w((lambda *s: torch.randn(*s, dtype=torch.bfloat16) * 0.05)(B, S, D)), 'wq': _w((lambda *s: torch.randn(*s, dtype=torch.bfloat16) * 0.05)(D, H * Dh)), 'wk': _w((lambda *s: torch.randn(*s, dtype=torch.bfloat16) * 0.05)(D, Dh)), 'wv': _w((lambda *s: torch.randn(*s, dtype=torch.bfloat16) * 0.05)(D, Dh)), 'wo': _w((lambda *s: torch.randn(*s, dtype=torch.bfloat16) * 0.05)(H * Dh, D))}
    _A_in = _A_setup()

    def _B_setup():
        B, S, D, F = (1, 2048, 576, 4608)
        return {'x': _w((lambda *s: torch.randn(*s, dtype=torch.bfloat16) * 0.05)(B, S, D)), 'W_gate': _w((lambda *s: torch.randn(*s, dtype=torch.bfloat16) * 0.8)(D, F) * (2.0 / D) ** 0.5), 'W_up': _w((lambda *s: torch.randn(*s, dtype=torch.bfloat16) * 0.8)(D, F) * (2.0 / D) ** 0.5), 'W_down': _w((lambda *s: torch.randn(*s, dtype=torch.bfloat16) * 0.8)(F, D) * (1.0 / F) ** 0.5)}
    _B_in = _B_setup()
    return {'A_x': _A_in['x'], 'A_wq': _A_in['wq'], 'A_wk': _A_in['wk'], 'A_wv': _A_in['wv'], 'A_wo': _A_in['wo'], 'B_x': _B_in['x'], 'B_W_gate': _B_in['W_gate'], 'B_W_up': _B_in['W_up'], 'B_W_down': _B_in['W_down'], 'c': _w((lambda *s: torch.randn(*s, dtype=torch.bfloat16) * 0.2)(1, 576) * 8.0), 'w_mod': _w((lambda *s: torch.randn(*s, dtype=torch.bfloat16) * 0.2)(576, 6 * 576) * 576 ** (-0.5) * 8.0)}

def reference(A_x, A_wq, A_wk, A_wv, A_wo, B_x, B_W_gate, B_W_up, B_W_down, c, w_mod):

    def _A_ref(x, wq, wk, wv, wo):
        B, S, D = x.shape
        H = 32
        Dh = D // H
        q = (lambda x, *d: x.permute(*d))((x @ wq).reshape(B, S, H, Dh), 0, 2, 1, 3)
        k = x @ wk
        v = x @ wv
        scores = torch.einsum('bhqd,bkd->bhqk', q, k) * Dh ** (-0.5)
        p = _softmax(scores, axis=-1)
        o = torch.einsum('bhqk,bkd->bhqd', p, v)
        return (lambda x, *d: x.permute(*d))(o, 0, 2, 1, 3).reshape(B, S, H * Dh) @ wo

    def _B_ref(x, W_gate, W_up, W_down):
        gate = torch.einsum('bsd,df->bsf', x, W_gate)
        up = torch.einsum('bsd,df->bsf', x, W_up)
        gate = gate * (lambda x, dt: x.to(dt))(gate > 0, gate.dtype)
        h = gate * up
        return torch.einsum('bsf,fd->bsd', h, W_down)
    x = A_x
    mod = (_silu(c) @ w_mod).reshape(c.shape[0], 6, 1, -1)
    sa, ca, ga, sb, cb, gb = (mod[:, 0].clone(), mod[:, 1].clone(), mod[:, 2].clone(), mod[:, 3].clone(), mod[:, 4].clone(), mod[:, 5].clone())
    h = _layernorm(x, axis=-1) * (1 + ca) + sa
    x = x + ga * _A_ref(h, A_wq, A_wk, A_wv, A_wo)
    h = _layernorm(x, axis=-1) * (1 + cb) + sb
    x = x + gb * _B_ref(h, B_W_gate, B_W_up, B_W_down)
    return x
```

```cpp
#include <hip/hip_runtime.h>
#include <hip/hip_bf16.h>
#include <stdint.h>

typedef unsigned short u16;
typedef short bf16x8 __attribute__((ext_vector_type(8)));
typedef short bf16x4 __attribute__((ext_vector_type(4)));
typedef float f32x2 __attribute__((ext_vector_type(2)));
typedef float f32x4 __attribute__((ext_vector_type(4)));
typedef float f32x16 __attribute__((ext_vector_type(16)));
typedef unsigned u32x2 __attribute__((ext_vector_type(2)));
typedef unsigned u32x4 __attribute__((ext_vector_type(4)));
typedef __bf16 bf2_t __attribute__((ext_vector_type(2)));
#define LAS __attribute__((address_space(3)))
#define DI __device__ __forceinline__
#define SBAR() __builtin_amdgcn_sched_barrier(0)
#define PIN(x) asm volatile("" : "+v"(x))
#define MFMA16(a, b, c) __builtin_amdgcn_mfma_f32_16x16x32_bf16(a, b, c, 0, 0, 0)
#define MFMA32(a, b, c) __builtin_amdgcn_mfma_f32_32x32x16_bf16(a, b, c, 0, 0, 0)
#define MFMA32K8(a, b, c) __builtin_amdgcn_mfma_f32_32x32x8bf16_1k(a, b, c, 0, 0, 0)

constexpr int S = 2048, D = 576, NH = 32, DH = 18, F = 4608;
constexpr float LN_EPS = 1e-5f;

constexpr size_t ACT_B = (size_t)S * D * 2;
constexpr int KV_TILE = 2496;
constexpr size_t OFF_MODF = 0;
constexpr size_t OFF_MODB = 16384;
constexpr size_t OFF_N1 = 32768;
constexpr size_t OFF_WQT = OFF_N1 + ACT_B;
constexpr size_t WT_B = 8 * 18 * 5 * 1024;
constexpr size_t OFF_WOT = OFF_WQT + WT_B;
constexpr size_t OFF_Q1 = OFF_WOT + WT_B;
constexpr size_t OFF_Q2 = OFF_Q1 + (size_t)NH * S * 32;
constexpr size_t OFF_KV = OFF_Q2 + (size_t)NH * S * 4;
constexpr size_t OFF_OT = OFF_KV + 160 * 1024;
constexpr size_t OFF_X1 = OFF_OT + ACT_B;
constexpr size_t OFF_Y = OFF_X1 + ACT_B;
constexpr size_t OFF_ST = OFF_Y + ACT_B;
constexpr size_t OFF_UVP = OFF_ST + (size_t)S * 64;
constexpr size_t OFF_UV = OFF_UVP + (size_t)18 * 9216 * 8;
constexpr size_t OFF_W1T = OFF_UV + (size_t)9216 * 8;
constexpr size_t OFF_W2T = OFF_W1T + (size_t)144 * 18 * 4096;
constexpr size_t OFF_HT = OFF_W2T + (size_t)8 * 144 * 4608;
constexpr size_t WS_END = OFF_HT + (size_t)S * F * 2;

DI float bf_lo(unsigned w) { return __uint_as_float(w << 16); }
DI float bf_hi(unsigned w) { return __uint_as_float(w & 0xffff0000u); }
DI float bf2f(u16 b) { return __uint_as_float((unsigned)b << 16); }
DI unsigned pk2(float lo, float hi) { f32x2 v = {lo, hi}; return __builtin_bit_cast(unsigned, __builtin_convertvector(v, bf2_t)); }
DI float rbf(float x) { return bf_lo(pk2(x, x)); }
DI unsigned plo(unsigned a, unsigned b) { return __builtin_amdgcn_perm(b, a, 0x05040100u); }
DI unsigned phi(unsigned a, unsigned b) { return __builtin_amdgcn_perm(b, a, 0x07060302u); }
DI int wave_id() { return __builtin_amdgcn_readfirstlane(threadIdx.x >> 6); }

template <int N> DI void glds(const void* sbase, unsigned voff, unsigned lds) {
  unsigned keep;
  if constexpr (N == 1)
    asm volatile("s_mov_b32 %0, m0\n\ts_mov_b32 m0, %3\n\ts_nop 2\n\tglobal_load_lds_dwordx4 %1, %2\n\ts_mov_b32 m0, %0"
                 : "=&s"(keep) : "v"(voff), "s"(sbase), "s"(lds) : "memory");
  if constexpr (N == 2)
    asm volatile("s_mov_b32 %0, m0\n\ts_mov_b32 m0, %3\n\ts_nop 2\n\tglobal_load_lds_dwordx4 %1, %2\n\tglobal_load_lds_dwordx4 %1, %2 offset:1024\n\ts_mov_b32 m0, %0"
                 : "=&s"(keep) : "v"(voff), "s"(sbase), "s"(lds) : "memory");
  if constexpr (N == 4)
    asm volatile("s_mov_b32 %0, m0\n\ts_mov_b32 m0, %3\n\ts_nop 2\n\tglobal_load_lds_dwordx4 %1, %2\n\tglobal_load_lds_dwordx4 %1, %2 offset:1024\n\t"
                 "global_load_lds_dwordx4 %1, %2 offset:2048\n\tglobal_load_lds_dwordx4 %1, %2 offset:3072\n\ts_mov_b32 m0, %0"
                 : "=&s"(keep) : "v"(voff), "s"(sbase), "s"(lds) : "memory");
}
#define GLD16(dst, voff, sbase, imm) asm volatile("s_nop 4\n\tglobal_load_dwordx4 %0, %1, %2 offset:%3" : "=v"(dst) : "v"(voff), "s"(sbase), "i"(imm) : "memory")
#define GLD16N(dst, voff, sbase, imm) asm volatile("global_load_dwordx4 %0, %1, %2 offset:%3" : "=v"(dst) : "v"(voff), "s"(sbase), "i"(imm) : "memory")
#define WAITV(n) asm volatile("s_waitcnt vmcnt(%0)" ::"i"(n) : "memory")
#define WAITV_BAR(n) asm volatile("s_waitcnt vmcnt(%0) lgkmcnt(0)\n\ts_barrier" ::"i"(n) : "memory")
#define LDS_BAR() asm volatile("s_waitcnt lgkmcnt(0)\n\ts_barrier" ::: "memory")

DI void quad_transpose(const u32x2 (&L)[8], u32x4 (&T)[4]) {
#pragma unroll
  for (int m = 0; m < 4; ++m) {
    T[0][m] = plo(L[2 * m][0], L[2 * m + 1][0]);
    T[1][m] = phi(L[2 * m][0], L[2 * m + 1][0]);
    T[2][m] = plo(L[2 * m][1], L[2 * m + 1][1]);
    T[3][m] = phi(L[2 * m][1], L[2 * m + 1][1]);
  }
}
DI int ofeat(int P, int j) {
  const int c = P / 9, i = P - 9 * c;
  return i < 8 ? 18 * (4 * c + (i >> 1)) + 8 * (i & 1) + j : 18 * (4 * c + (j >> 1)) + 16 + (j & 1);
}

constexpr int P_GEMV = 216, P_LN = 86, P_WQ = 24, P_WO = 24, P_CONST = 1;
__global__ __launch_bounds__(384) void k_prep(const u16* __restrict__ x, const u16* __restrict__ wq, const u16* __restrict__ wk,
                                             const u16* __restrict__ wv, const u16* __restrict__ wo, const u16* __restrict__ cvec,
                                             const u16* __restrict__ wmod, char* __restrict__ ws) {
  __shared__ float red[6][16];
  const int tid = threadIdx.x, lane = tid & 63, w = tid >> 6;
  int b = blockIdx.x;
  if (b < P_GEMV) {
    const int strip = ((b >> 5) * 8 + (b & 7)) * 4 + ((b >> 3) & 3);
    const int sidx = (b < 192) ? strip : b;
    const int ch = tid & 1, r0 = tid >> 1;
    u32x4 wv4[3]; float sc[3];
#pragma unroll
    for (int p = 0; p < 3; ++p) wv4[p] = *(const u32x4*)(wmod + (size_t)(r0 + 192 * p) * 3456 + sidx * 16 + ch * 8);
#pragma unroll
    for (int p = 0; p < 3; ++p) { const float cv = bf2f(cvec[r0 + 192 * p]); sc[p] = rbf(cv / (1.f + __expf(-cv))); }
    float acc[8];
#pragma unroll
    for (int j = 0; j < 4; ++j) {
      acc[2 * j] = sc[0] * bf_lo(wv4[0][j]) + sc[1] * bf_lo(wv4[1][j]) + sc[2] * bf_lo(wv4[2][j]);
      acc[2 * j + 1] = sc[0] * bf_hi(wv4[0][j]) + sc[1] * bf_hi(wv4[1][j]) + sc[2] * bf_hi(wv4[2][j]);
    }
#pragma unroll
    for (int o = 2; o < 64; o <<= 1)
#pragma unroll
      for (int j = 0; j < 8; ++j) acc[j] += __shfl_xor(acc[j], o);
    if (lane < 2)
#pragma unroll
      for (int j = 0; j < 8; ++j) red[w][lane * 8 + j] = acc[j];
    __syncthreads();
    if (tid < 16) {
      float s = red[0][tid] + red[1][tid] + red[2][tid] + red[3][tid] + red[4][tid] + red[5][tid];
      s = rbf(s);
      const int col = sidx * 16 + tid, which = col / D;
      if (which == 1 || which == 4) s = rbf(1.f + s);
      ((float*)(ws + OFF_MODF))[col] = s;
      ((u16*)(ws + OFF_MODB))[col] = (u16)(__float_as_uint(s) >> 16);
    }
    return;
  }
  b -= P_GEMV;
  if (b < P_LN) {
    const int wv_ = b * 6 + w;
    if (wv_ >= 512) return;
    const int token = wv_ * 4 + (lane >> 4), sub = lane & 15;
    u32x2 xv[9];
#pragma unroll
    for (int i = 0; i < 9; ++i) xv[i] = *(const u32x2*)(x + (size_t)token * D + (sub + 16 * i) * 4);
    float v[36], s = 0.f;
#pragma unroll
    for (int i = 0; i < 9; ++i) { v[4 * i] = bf_lo(xv[i][0]); v[4 * i + 1] = bf_hi(xv[i][0]); v[4 * i + 2] = bf_lo(xv[i][1]); v[4 * i + 3] = bf_hi(xv[i][1]); }
#pragma unroll
    for (int i = 0; i < 36; ++i) s += v[i];
#pragma unroll
    for (int o = 1; o < 16; o <<= 1) s += __shfl_xor(s, o);
    const float mean = s * (1.f / D);
    float q = 0.f;
#pragma unroll
    for (int i = 0; i < 36; ++i) { v[i] -= mean; q += v[i] * v[i]; }
#pragma unroll
    for (int o = 1; o < 16; o <<= 1) q += __shfl_xor(q, o);
    const float rstd = rsqrtf(q * (1.f / D) + LN_EPS);
    char* dst = ws + OFF_N1 + (size_t)(token >> 4) * (72 * 256) + (token & 15) * 16;
#pragma unroll
    for (int i = 0; i < 9; ++i) {
      const int hp = sub + 16 * i;
      u32x2 o2 = {pk2(v[4 * i] * rstd, v[4 * i + 1] * rstd), pk2(v[4 * i + 2] * rstd, v[4 * i + 3] * rstd)};
      *(u32x2*)(dst + (hp >> 1) * 256 + (hp & 1) * 8) = o2;
    }
    return;
  }
  b -= P_LN;
  const int r = lane & 15, g = lane >> 4;
  if (b < P_WQ + P_WO) {
    const bool isq = b < P_WQ;
    const int unit = (isq ? b : b - P_WQ) * 6 + w;
    const int c = unit & 7, kt = unit >> 3;
    const u16* W = isq ? wq : wo;
    u32x2 L[8]; u16 E[8];
    const int col = isq ? 18 * (4 * c + (r >> 2)) + 4 * (r & 3) : 72 * c + 4 * r;
#pragma unroll
    for (int j = 0; j < 8; ++j) {
      const int k = isq ? 32 * kt + 8 * g + j : ofeat(4 * kt + g, j);
      const unsigned* p = (const unsigned*)(W + (size_t)k * D + col);
      L[j][0] = p[0]; L[j][1] = p[1];
      u16 e = 0;
      if (isq) {
        if (r < 8) e = wq[(size_t)k * D + 18 * (4 * c + (r >> 1)) + 16 + (r & 1)];
        else {
          const int rr = r - 8;
          if (c < 2) e = wk[k * DH + 8 * c + rr];
          else if (c == 2) { if (rr < 2) e = wk[k * DH + 16 + rr]; else if (rr >= 4 && rr < 6) e = wv[k * DH + 12 + rr]; }
          else if (c < 5) e = wv[k * DH + 8 * (c - 3) + rr];
        }
      } else if (r < 8) e = wo[(size_t)k * D + 72 * c + 64 + r];
      E[j] = e;
    }
    u32x4 T[4];
    quad_transpose(L, T);
    char* dst = ws + (isq ? OFF_WQT : OFF_WOT) + (size_t)(c * 18 + kt) * 5120 + lane * 16;
#pragma unroll
    for (int e = 0; e < 4; ++e) *(u32x4*)(dst + e * 1024) = T[e];
    u32x4 t4 = {E[0] | ((unsigned)E[1] << 16), E[2] | ((unsigned)E[3] << 16), E[4] | ((unsigned)E[5] << 16), E[6] | ((unsigned)E[7] << 16)};
    *(u32x4*)(dst + 4096) = t4;
    return;
  }
  if (tid < 256) {
    const int tile = tid >> 2, sh = tid & 3;
    const u32x4 ones = {0x3f803f80u, 0x3f803f80u, 0x3f803f80u, 0x3f803f80u};
    *(u32x4*)(ws + OFF_KV + (size_t)tile * KV_TILE + 1024 + sh * 304 + 18 * 16) = ones;
  }
}

constexpr int SG_LDS = 92160 + 5120;
template <bool MOD>
DI void small_gemm(const char* act, const char* wt, const char* modf, int tb, int c, char* smem, f32x4 (&acc)[5]) {
  const int lane = threadIdx.x & 63, w = wave_id(), tt = w & 3, kh = w >> 2, g = lane >> 4;
  const unsigned voff = lane * 16, lds0 = (unsigned)(uintptr_t)smem;
  const char* wsrc = wt + (size_t)c * 92160;
  const char* asrc = act + (size_t)(tb * 4 + tt) * (72 * 256) + kh * 9216;
  bf16x8 a[9];
  const int pofs = kh * 46080 + (tt == 3 ? 11 : 4 * tt) * 1024;
  if (MOD) glds<1>(modf + (w < 4 ? w : 4) * 1024, voff, lds0 + 92160 + (w < 4 ? w : 4) * 1024);
  glds<4>(wsrc + pofs, voff, lds0 + pofs);
  GLD16(a[0], voff, asrc, 0); GLD16N(a[1], voff, asrc, 1024); GLD16N(a[2], voff, asrc, 2048);
  glds<4>(wsrc + pofs + 15360, voff, lds0 + pofs + 15360);
  { const char* p = asrc + 3072; GLD16(a[3], voff, p, 0); GLD16N(a[4], voff, p, 1024); GLD16N(a[5], voff, p, 2048); }
  glds<4>(wsrc + pofs + 30720, voff, lds0 + pofs + 30720);
  { const char* p = asrc + 6144; GLD16(a[6], voff, p, 0); GLD16N(a[7], voff, p, 1024); GLD16N(a[8], voff, p, 2048); }
#pragma unroll
  for (int e = 0; e < 5; ++e) acc[e] = f32x4{0.f, 0.f, 0.f, 0.f};
  const LAS char* wl = (const LAS char*)smem + kh * 46080 + lane * 16;
  const LAS char* ml = (const LAS char*)smem + 92160 + (kh * 288 + g * 8) * 4;
#define SG_STEP(i)                                                                                   \
  {                                                                                                  \
    bf16x8 bfr = a[i];                                                                               \
    if (MOD) {                                                                                       \
      const f32x4 s0 = *(const LAS f32x4*)(ml + (i) * 128), s1 = *(const LAS f32x4*)(ml + (i) * 128 + 16);                 \
      const f32x4 c0 = *(const LAS f32x4*)(ml + 2304 + (i) * 128), c1 = *(const LAS f32x4*)(ml + 2304 + (i) * 128 + 16);   \
      const u32x4 n = __builtin_bit_cast(u32x4, bfr);                                                \
      u32x4 h;                                                                                       \
      h[0] = pk2(bf_lo(n[0]) * c0[0] + s0[0], bf_hi(n[0]) * c0[1] + s0[1]);                          \
      h[1] = pk2(bf_lo(n[1]) * c0[2] + s0[2], bf_hi(n[1]) * c0[3] + s0[3]);                          \
      h[2] = pk2(bf_lo(n[2]) * c1[0] + s1[0], bf_hi(n[2]) * c1[1] + s1[1]);                          \
      h[3] = pk2(bf_lo(n[3]) * c1[2] + s1[2], bf_hi(n[3]) * c1[3] + s1[3]);                          \
      bfr = __builtin_bit_cast(bf16x8, h);                                                           \
    }                                                                                                \
    _Pragma("unroll") for (int e = 0; e < 5; ++e)                                                    \
      acc[e] = MFMA16(*(const LAS bf16x8*)(wl + (i) * 5120 + e * 1024), bfr, acc[e]);                \
  }
  asm volatile("s_waitcnt vmcnt(14)\n\ts_barrier" : "+v"(a[0]), "+v"(a[1]), "+v"(a[2])::"memory");
  SG_STEP(0) SG_STEP(1) SG_STEP(2)
  asm volatile("s_waitcnt vmcnt(7)\n\ts_barrier" : "+v"(a[3]), "+v"(a[4]), "+v"(a[5])::"memory");
  SG_STEP(3) SG_STEP(4) SG_STEP(5)
  asm volatile("s_waitcnt vmcnt(0)\n\ts_barrier" : "+v"(a[6]), "+v"(a[7]), "+v"(a[8])::"memory");
  SG_STEP(6) SG_STEP(7) SG_STEP(8)
#undef SG_STEP
  LAS char* rl = (LAS char*)smem + (tt & 1) * 5120 + (tt >> 1) * 46080 + lane * 16;
  if (kh == 1)
#pragma unroll
    for (int e = 0; e < 5; ++e) *(LAS f32x4*)(rl + e * 1024) = acc[e];
  LDS_BAR();
  if (kh == 0)
#pragma unroll
    for (int e = 0; e < 5; ++e) acc[e] += *(const LAS f32x4*)(rl + e * 1024);
}

__global__ __launch_bounds__(512) void k_qkv(char* __restrict__ ws) {
  extern __shared__ __attribute__((aligned(16))) char smem[];
  const int b = blockIdx.x, c = b & 7, tb = b >> 3;
  f32x4 acc[5];
  small_gemm<true>(ws + OFF_N1, ws + OFF_WQT, ws + OFF_MODF, tb, c, smem, acc);
  const int lane = threadIdx.x & 63, w = threadIdx.x >> 6;
  if (w >= 4) return;
  const int g = lane >> 4, token = tb * 64 + w * 16 + (lane & 15);
  constexpr float QS = 0.23570226039551584f * 1.4426950408889634f;
  u32x4 q0, q1;
  q0[0] = pk2(acc[0][0] * QS, acc[1][0] * QS); q0[1] = pk2(acc[2][0] * QS, acc[3][0] * QS);
  q0[2] = pk2(acc[0][1] * QS, acc[1][1] * QS); q0[3] = pk2(acc[2][1] * QS, acc[3][1] * QS);
  q1[0] = pk2(acc[0][2] * QS, acc[1][2] * QS); q1[1] = pk2(acc[2][2] * QS, acc[3][2] * QS);
  q1[2] = pk2(acc[0][3] * QS, acc[1][3] * QS); q1[3] = pk2(acc[2][3] * QS, acc[3][3] * QS);
  char* qd = ws + OFF_Q1 + ((size_t)(4 * c + g) * S + token) * 32;
  *(u32x4*)qd = q0; *(u32x4*)(qd + 16) = q1;
  const f32x4 t = acc[4];
  if (g < 2) {
    unsigned* q2 = (unsigned*)(ws + OFF_Q2);
    q2[(size_t)(4 * c + 2 * g) * S + token] = pk2(t[0] * QS, t[1] * QS);
    q2[(size_t)(4 * c + 2 * g + 1) * S + token] = pk2(t[2] * QS, t[3] * QS);
  } else {
    char* kv = ws + OFF_KV + (size_t)(token >> 5) * KV_TILE;
    const int key = token & 31, k16 = token & 15;
    char* vrow = kv + 1024 + (key >> 4) * 608 + ((k16 >> 2) & 1) * 304 + ((k16 & 3) + 4 * (k16 >> 3)) * 2;
    const unsigned p0 = pk2(t[0], t[1]), p1 = pk2(t[2], t[3]);
    if (c < 2) {
      *(u32x2*)(kv + (key + 32 * c) * 16 + (g - 2) * 8) = u32x2{p0, p1};
    } else if (c == 2) {
      if (g == 2) *(u32x2*)(kv + 2240 + key * 8) = u32x2{p0, 0x00003f80u};
      else { *(u16*)(vrow + 16 * 16) = (u16)p0; *(u16*)(vrow + 17 * 16) = (u16)(p0 >> 16); }
    } else if (c < 5) {
      char* vr = vrow + (4 * (c - 3) + 8 * (g - 2)) * 16;
      *(u16*)(vr) = (u16)p0; *(u16*)(vr + 16) = (u16)(p0 >> 16); *(u16*)(vr + 32) = (u16)p1; *(u16*)(vr + 48) = (u16)(p1 >> 16);
    }
  }
}

constexpr int ATT_LDS = 64 * KV_TILE;
__global__ __launch_bounds__(1024) void k_attn(const u16* __restrict__ wg, const u16* __restrict__ wu, const u16* __restrict__ wd,
                                              char* __restrict__ ws) {
  extern __shared__ __attribute__((aligned(16))) char smem[];
  const int lane = threadIdx.x & 63, w = wave_id(), b = blockIdx.x, c = b & 7, tb = b >> 3;
  const int r = lane & 31, h = lane >> 5, qt = w & 7, hk = w >> 3;
  const unsigned voff = lane * 16, lds0 = (unsigned)(uintptr_t)smem;
  const char* kvg = ws + OFF_KV;
  glds<1>(kvg + w * 1024, voff, lds0 + w * 1024);
  const int head = 4 * c + (qt >> 1), token = tb * 64 + (qt & 1) * 32 + r;
  bf16x8 q1; unsigned q2w;
  {
    const char* qp = ws + OFF_Q1 + ((size_t)head * S + tb * 64 + (qt & 1) * 32) * 32;
    const unsigned qo = r * 32 + h * 16;
    GLD16(q1, qo, qp, 0);
    const char* qp2 = ws + OFF_Q2 + ((size_t)head * S + tb * 64 + (qt & 1) * 32) * 4;
    const unsigned qo2 = r * 4;
    asm volatile("s_nop 4\n\tglobal_load_dword %0, %1, %2" : "=v"(q2w) : "v"(qo2), "s"(qp2) : "memory");
  }
#pragma unroll
  for (int i = 1; i < 10; ++i) {
    int p = i * 16 + w;
    if (i == 9) p = p > 155 ? 155 : p;
    glds<1>(kvg + p * 1024, voff, lds0 + p * 1024);
  }
  const LAS char* kp = (const LAS char*)smem + hk * KV_TILE + lane * 16;
  const LAS char* vp = (const LAS char*)smem + hk * KV_TILE + 1024 + h * 304 + (r < 18 ? r : 18) * 16;
  const LAS char* k2p = (const LAS char*)smem + hk * KV_TILE + 2240 + r * 8;
  constexpr int TS = 2 * KV_TILE;
  asm volatile("s_waitcnt vmcnt(9)\n\ts_barrier" : "+v"(q1), "+v"(q2w)::"memory");
  if (h) q2w = 0;
  const f32x16 zero16 = {};
  f32x16 sA, sB, o = {};
  u32x4 p0, p1 = {};
  bf16x8 ka, v0, v1 = {};
  bf16x4 kb, q2;
  float mshift;
  {
    ka = *(const LAS bf16x8*)kp; kb = *(const LAS bf16x4*)k2p;
    q2 = __builtin_bit_cast(bf16x4, u32x2{q2w, 0u});
    sA = MFMA32K8(kb, q2, zero16); sA = MFMA32(ka, q1, sA);
    float m = fmaxf(sA[0], sA[1]);
#pragma unroll
    for (int i = 2; i < 16; i += 2) m = fmaxf(fmaxf(m, sA[i]), sA[i + 1]);
    m = fmaxf(m, __shfl_xor(m, 32));
    mshift = rbf(m + 64.f);
    q2 = __builtin_bit_cast(bf16x4, u32x2{q2w, h ? 0u : (__float_as_uint(-mshift) >> 16)});
    sA = MFMA32K8(kb, q2, zero16); sA = MFMA32(ka, q1, sA);
  }
#define EX(x) x = __builtin_amdgcn_exp2f(x)
#define ATT_STEP(C, N, off, LAST)                                                                         \
  {                                                                                                       \
    if (!(LAST)) { ka = *(const LAS bf16x8*)(kp + (off) + TS); kb = *(const LAS bf16x4*)(k2p + (off) + TS); } \
    v0 = *(const LAS bf16x8*)(vp + (off));                                                                \
    SBAR();                                                                                               \
    EX(C[0]); EX(C[1]);                                                                                   \
    o = MFMA32(v1, __builtin_bit_cast(bf16x8, p1), o);                                                    \
    SBAR();                                                                                               \
    v1 = *(const LAS bf16x8*)(vp + (off) + 608);                                                          \
    EX(C[2]); EX(C[3]); p0[0] = pk2(C[0], C[1]); EX(C[4]); p0[1] = pk2(C[2], C[3]); EX(C[5]);             \
    PIN(p0); PIN(C); SBAR();                                                                              \
    if (!(LAST)) N = MFMA32K8(kb, q2, zero16);                                                            \
    SBAR();                                                                                               \
    EX(C[6]); EX(C[7]); p0[2] = pk2(C[4], C[5]); EX(C[8]); p0[3] = pk2(C[6], C[7]); EX(C[9]);             \
    PIN(p0); PIN(C); SBAR();                                                                              \
    if (!(LAST)) N = MFMA32(ka, q1, N);                                                                   \
    SBAR();                                                                                               \
    EX(C[10]); EX(C[11]); p1[0] = pk2(C[8], C[9]); EX(C[12]); p1[1] = pk2(C[10], C[11]); EX(C[13]);       \
    PIN(p1); PIN(C); SBAR();                                                                              \
    o = MFMA32(v0, __builtin_bit_cast(bf16x8, p0), o);                                                    \
    SBAR();                                                                                               \
    EX(C[14]); EX(C[15]); p1[2] = pk2(C[12], C[13]); p1[3] = pk2(C[14], C[15]);                           \
    PIN(p1); SBAR();                                                                                      \
  }
#define ATT_PAIR(off) ATT_STEP(sA, sB, off, false) ATT_STEP(sB, sA, (off) + TS, false)
  ATT_PAIR(0)
  WAITV_BAR(7);
  ATT_PAIR(2 * TS) ATT_PAIR(4 * TS) ATT_PAIR(6 * TS)
  WAITV_BAR(0);
  const int uid = b * 16 + w;
  const int r16 = lane & 15, g = lane >> 4;
  u32x2 L[8];
  u32x4 mv = {};
  if (uid < 2592) {
    const int s = (uid / 36) * 2 + (uid & 1), kt = (uid % 36) >> 1;
    const u16* W = ((r16 >> 1) & 1) ? wu : wg;
    const u16* src = W + (size_t)(32 * kt + 8 * g) * F + 32 * s + 4 * ((r16 >> 2) * 2 + (r16 & 1));
#pragma unroll
    for (int j = 0; j < 8; ++j) L[j] = *(const u32x2*)(src + (size_t)j * F);
    if ((r16 & 3) < 2) mv = *(const u32x4*)(ws + OFF_MODB + (((r16 & 1) ? 3 : 4) * D + 32 * kt + 8 * g) * 2);
  } else if (uid < 3744) {
    const int id = uid - 2592, kt = id >> 3, cc = id & 7;
    const u16* src = wd + (size_t)(32 * kt + 8 * g) * D + 72 * cc + 4 * r16;
#pragma unroll
    for (int j = 0; j < 8; ++j) L[j] = *(const u32x2*)(src + (size_t)j * D);
  } else if (uid < 3744 + 288) {
    const int id = uid - 3744, kt = id >> 1, c0 = (id & 1) * 4 + (r16 >> 3);
    const u16* src = wd + (size_t)(32 * kt + 8 * g) * D + 72 * c0 + 64 + (r16 & 7);
#pragma unroll
    for (int j = 0; j < 8; ++j) { L[j][0] = src[(size_t)j * D]; L[j][1] = src[(size_t)j * D + 144]; }
  } else {
#pragma unroll
    for (int j = 0; j < 8; ++j) L[j] = u32x2{0u, 0u};
  }
  SBAR();
  kp += 8 * TS; vp += 8 * TS; k2p += 8 * TS;
  ATT_PAIR(0) ATT_PAIR(2 * TS) ATT_PAIR(4 * TS) ATT_PAIR(6 * TS)
  {
    u32x4 T[4];
    quad_transpose(L, T);
    if (uid < 2592) {
      const int s = (uid / 36) * 2 + (uid & 1), kt = (uid % 36) >> 1;
      char* dst = ws + OFF_W1T + (size_t)(s * 18 + kt) * 4096 + lane * 16;
      f32x2 uv = {0.f, 0.f};
#pragma unroll
      for (int e = 0; e < 4; ++e) {
        __builtin_nontemporal_store(T[e], (u32x4*)(dst + e * 1024));
        const f32x4 d4 = MFMA16(__builtin_bit_cast(bf16x8, mv), __builtin_bit_cast(bf16x8, T[e]), (f32x4{0.f, 0.f, 0.f, 0.f}));
        if (g == e) uv = f32x2{d4[0], d4[1]};
      }
      *(f32x2*)(ws + OFF_UVP + ((size_t)kt * 9216 + s * 64 + lane) * 8) = uv;
    } else if (uid < 3744) {
      const int id = uid - 2592, kt = id >> 3, cc = id & 7;
      char* dst = ws + OFF_W2T + (size_t)(cc * 144 + kt) * 4608 + lane * 16;
#pragma unroll
      for (int e = 0; e < 4; ++e) __builtin_nontemporal_store(T[e], (u32x4*)(dst + e * 1024));
    } else if (uid < 3744 + 288) {
      const int id = uid - 3744, kt = id >> 1, c0 = (id & 1) * 4 + (r16 >> 3);
      char* dst = ws + OFF_W2T + (size_t)(c0 * 144 + kt) * 4608 + 4096 + g * 128 + (r16 & 7) * 16;
      *(u32x4*)dst = T[0];
      *(u32x4*)(dst + (size_t)2 * 144 * 4608) = T[2];
    }
  }
  SBAR();
  kp += 8 * TS; vp += 8 * TS; k2p += 8 * TS;
  ATT_PAIR(0) ATT_PAIR(2 * TS) ATT_PAIR(4 * TS) ATT_PAIR(6 * TS)
  kp += 8 * TS; vp += 8 * TS; k2p += 8 * TS;
  ATT_PAIR(0) ATT_PAIR(2 * TS) ATT_PAIR(4 * TS)
  ATT_STEP(sA, sB, 6 * TS, false) ATT_STEP(sB, sA, 7 * TS, true)
  o = MFMA32(v1, __builtin_bit_cast(bf16x8, p1), o);
#undef ATT_PAIR
#undef ATT_STEP
  {
    float amax = fabsf(o[0]);
#pragma unroll
    for (int i = 1; i < 11; ++i) amax = fmaxf(amax, fabsf(o[i]));
    bool ok = (o[10] >= 0x1p-100f) && (amax < 0x1p126f);
#pragma unroll
    for (int i = 0; i < 10; ++i) ok = ok && (o[i] == o[i]);
    if (__builtin_expect(!__all(ok), 0)) {
      kp -= 24 * TS; vp -= 24 * TS; k2p -= 24 * TS;
      q2 = __builtin_bit_cast(bf16x4, u32x2{q2w, 0u});
      float m = -INFINITY;
      o = zero16;
#pragma unroll 1
      for (int t = 0; t < 32; ++t) {
        ka = *(const LAS bf16x8*)(kp + t * TS); kb = *(const LAS bf16x4*)(k2p + t * TS);
        v0 = *(const LAS bf16x8*)(vp + t * TS); v1 = *(const LAS bf16x8*)(vp + t * TS + 608);
        sA = MFMA32K8(kb, q2, zero16); sA = MFMA32(ka, q1, sA);
        float mt = m;
#pragma unroll
        for (int i = 0; i < 16; ++i) mt = fmaxf(mt, sA[i]);
        mt = fmaxf(mt, __shfl_xor(mt, 32));
        const float al = (m == mt) ? 1.f : __builtin_amdgcn_exp2f(m - mt);
        m = mt;
#pragma unroll
        for (int i = 0; i < 16; ++i) { o[i] *= al; sA[i] = __builtin_amdgcn_exp2f(sA[i] - m); }
#pragma unroll
        for (int i = 0; i < 4; ++i) { p0[i] = pk2(sA[2 * i], sA[2 * i + 1]); p1[i] = pk2(sA[8 + 2 * i], sA[9 + 2 * i]); }
        o = MFMA32(v0, __builtin_bit_cast(bf16x8, p0), o);
        o = MFMA32(v1, __builtin_bit_cast(bf16x8, p1), o);
      }
      mshift = m;
    }
  }
#undef EX
  LDS_BAR();
  LAS char* cl = (LAS char*)smem + qt * 3072 + lane * 16;
  if (hk) {
    *(LAS f32x4*)(cl) = f32x4{o[0], o[1], o[2], o[3]};
    *(LAS f32x4*)(cl + 1024) = f32x4{o[4], o[5], o[6], o[7]};
    *(LAS f32x4*)(cl + 2048) = f32x4{o[8], o[9], o[10], mshift};
  }
  LDS_BAR();
  if (!hk) {
    const f32x4 a0 = *(const LAS f32x4*)(cl), a1 = *(const LAS f32x4*)(cl + 1024), a2 = *(const LAS f32x4*)(cl + 2048);
    const float mm = fmaxf(mshift, a2[3]);
    const float fa = (mshift == mm) ? 1.f : __builtin_amdgcn_exp2f(mshift - mm);
    const float fb = (a2[3] == mm) ? 1.f : __builtin_amdgcn_exp2f(a2[3] - mm);
    const float l = fa * o[10] + fb * a2[2];
    const float ra = fa / l, rb = fb / l;
    u32x4 ov;
    ov[0] = pk2(o[0] * ra + a0[0] * rb, o[1] * ra + a0[1] * rb);
    ov[1] = pk2(o[2] * ra + a0[2] * rb, o[3] * ra + a0[3] * rb);
    ov[2] = pk2(o[4] * ra + a1[0] * rb, o[5] * ra + a1[1] * rb);
    ov[3] = pk2(o[6] * ra + a1[2] * rb, o[7] * ra + a1[3] * rb);
    char* od = ws + OFF_OT + (size_t)(token >> 4) * (72 * 256) + (token & 15) * 16 + 9 * c * 256;
    *(u32x4*)(od + (2 * (qt >> 1) + h) * 256) = ov;
    if (!h) *(unsigned*)(od + 8 * 256 + (qt >> 1) * 4) = pk2(o[8] * ra + a2[0] * rb, o[9] * ra + a2[1] * rb);
  }
}

__global__ __launch_bounds__(512) void k_oproj(const u16* __restrict__ x, char* __restrict__ ws) {
  extern __shared__ __attribute__((aligned(16))) char smem[];
  const int b = blockIdx.x;
  const int c = b & 7, tb = b >> 3;
  f32x4 acc[5];
  small_gemm<false>(ws + OFF_OT, ws + OFF_WOT, nullptr, tb, c, smem, acc);
  const int lane = threadIdx.x & 63, w = threadIdx.x >> 6;
  if (w >= 4) {
    if (w == 4 && lane < 36) {
      const int n = b * 36 + lane;
      const f32x2* p = (const f32x2*)(ws + OFF_UVP) + n;
      f32x2 s = {0.f, 0.f};
#pragma unroll
      for (int kt = 0; kt < 18; ++kt) s += p[(size_t)kt * 9216];
      float* uv = (float*)(ws + OFF_UV) + (n >> 4) * 32 + (n & 15);
      uv[0] = s[0]; uv[16] = s[1];
    }
    return;
  }
  const int g = lane >> 4, token = tb * 64 + w * 16 + (lane & 15);
  const float* modf = (const float*)(ws + OFF_MODF);
  const int d0 = 72 * c + 16 * g, d1 = 72 * c + 64 + 4 * (g & 1);
  const u32x4 xa = *(const u32x4*)(x + (size_t)token * D + d0), xb = *(const u32x4*)(x + (size_t)token * D + d0 + 8);
  const u32x2 xc = *(const u32x2*)(x + (size_t)token * D + d1);
  float v[20], cb[20];
#pragma unroll
  for (int i = 0; i < 4; ++i) {
    const f32x4 ga = *(const f32x4*)(modf + 2 * D + d0 + 4 * i), c4 = *(const f32x4*)(modf + 4 * D + d0 + 4 * i);
    const unsigned w0 = i < 2 ? xa[2 * i] : xb[2 * i - 4], w1 = i < 2 ? xa[2 * i + 1] : xb[2 * i - 3];
    v[4 * i] = rbf(bf_lo(w0) + ga[0] * acc[0][i]); v[4 * i + 1] = rbf(bf_hi(w0) + ga[1] * acc[1][i]);
    v[4 * i + 2] = rbf(bf_lo(w1) + ga[2] * acc[2][i]); v[4 * i + 3] = rbf(bf_hi(w1) + ga[3] * acc[3][i]);
    cb[4 * i] = c4[0]; cb[4 * i + 1] = c4[1]; cb[4 * i + 2] = c4[2]; cb[4 * i + 3] = c4[3];
  }
  {
    const f32x4 ga = *(const f32x4*)(modf + 2 * D + d1), c4 = *(const f32x4*)(modf + 4 * D + d1);
    v[16] = rbf(bf_lo(xc[0]) + ga[0] * acc[4][0]); v[17] = rbf(bf_hi(xc[0]) + ga[1] * acc[4][1]);
    v[18] = rbf(bf_lo(xc[1]) + ga[2] * acc[4][2]); v[19] = rbf(bf_hi(xc[1]) + ga[3] * acc[4][3]);
    cb[16] = c4[0]; cb[17] = c4[1]; cb[18] = c4[2]; cb[19] = c4[3];
  }
  const size_t base = (size_t)(token >> 4) * (72 * 256) + (token & 15) * 16 + 9 * c * 256;
  char* x1d = ws + OFF_X1 + base;
  char* yd = ws + OFF_Y + base;
#pragma unroll
  for (int p = 0; p < 2; ++p) {
    u32x4 a, y;
#pragma unroll
    for (int m = 0; m < 4; ++m) {
      a[m] = pk2(v[8 * p + 2 * m], v[8 * p + 2 * m + 1]);
      y[m] = pk2(v[8 * p + 2 * m] * cb[8 * p + 2 * m], v[8 * p + 2 * m + 1] * cb[8 * p + 2 * m + 1]);
    }
    *(u32x4*)(x1d + (2 * g + p) * 256) = a;
    *(u32x4*)(yd + (2 * g + p) * 256) = y;
  }
  if (g < 2) {
    *(u32x2*)(x1d + 8 * 256 + g * 8) = u32x2{pk2(v[16], v[17]), pk2(v[18], v[19])};
    *(u32x2*)(yd + 8 * 256 + g * 8) = u32x2{pk2(v[16] * cb[16], v[17] * cb[17]), pk2(v[18] * cb[18], v[19] * cb[19])};
  }
  float s = 0.f;
#pragma unroll
  for (int i = 0; i < 16; ++i) s += v[i];
  if (g < 2) s += v[16] + v[17] + v[18] + v[19];
  s += __shfl_xor(s, 16); s += __shfl_xor(s, 32);
  const float mean = s * (1.f / 72.f);
  float q = 0.f;
#pragma unroll
  for (int i = 0; i < 16; ++i) q += (v[i] - mean) * (v[i] - mean);
  if (g < 2)
#pragma unroll
    for (int i = 16; i < 20; ++i) q += (v[i] - mean) * (v[i] - mean);
  q += __shfl_xor(q, 16); q += __shfl_xor(q, 32);
  if (g == 0) *(f32x2*)(ws + OFF_ST + ((size_t)token * 8 + c) * 8) = f32x2{mean, q};
}

constexpr int G1_SLOT = 36864, G1_UV = 4 * G1_SLOT, G1_LDS = G1_UV + 3072;
__global__ __launch_bounds__(512, 2) void k_ffn1(char* __restrict__ ws) {
  extern __shared__ __attribute__((aligned(16))) char smem[];
  const int lane = threadIdx.x & 63, w = wave_id(), b = blockIdx.x, g = lane >> 4;
  const int n = (b & 7) * 4 + ((b >> 3) & 3), rb = b >> 5;
  const int odd = n & 1, qfull = (n >> 1) * 9 + (odd ? 5 : 0), qhalf = (n >> 1) * 9 + 4;
  const unsigned voff = lane * 16, lds0 = (unsigned)(uintptr_t)smem;
  const char* w1 = ws + OFF_W1T;
  const char* wfull = w1 + (size_t)qfull * 73728 + w * 4608;
  const char* whalf = w1 + (size_t)qhalf * 73728 + odd * 2048;
#define G1_STAGE(s)                                                                        \
  {                                                                                        \
    const unsigned ld = lds0 + ((s) & 3) * G1_SLOT + w * 4608;                             \
    if ((s) < 8) {                                                                         \
      glds<4>(wfull + (s) * G1_SLOT, voff, ld);                                            \
      if (lane < 32) glds<1>(wfull + (s) * G1_SLOT + 4096, voff, ld + 4096);               \
    } else {                                                                               \
      _Pragma("unroll") for (int i_ = 0; i_ < 5; ++i_) {                                   \
        const unsigned by = w * 4608 + i_ * 1024 + voff;                                   \
        const unsigned so = (by >> 11) * 4096 + (by & 2047);                               \
        if (i_ < 4 || lane < 32) glds<1>(whalf, so, ld + i_ * 1024);                       \
      }                                                                                    \
    }                                                                                      \
  }
  const char* ysrc = ws + OFF_Y + (size_t)(rb * 16 + w * 2) * (72 * 256);
  bf16x8 y0[18], y1[18];
  if (w == 0) glds<2>(ws + OFF_UV + qfull * 512, voff, lds0 + G1_UV);
  else if (w == 1) glds<2>(ws + OFF_UV + qfull * 512, voff, lds0 + G1_UV);
  else { if (lane < 16) { glds<1>(ws + OFF_UV + qhalf * 512 + odd * 256, voff, lds0 + G1_UV + 2048); glds<1>(ws + OFF_UV + qhalf * 512 + odd * 256, voff, lds0 + G1_UV + 2048); } }
  G1_STAGE(0)
#define G1_LDY(k0)                                                                                                        \
  {                                                                                                                       \
    const char* pa = ysrc + (k0) * 1024; const char* pb = pa + 72 * 256;                                                  \
    GLD16(y0[k0], voff, pa, 0); GLD16(y1[k0], voff, pb, 0); GLD16N(y0[(k0) + 1], voff, pa, 1024); GLD16N(y1[(k0) + 1], voff, pb, 1024); \
    GLD16N(y0[(k0) + 2], voff, pa, 2048); GLD16N(y1[(k0) + 2], voff, pb, 2048);                                           \
  }
  G1_LDY(0) G1_LDY(3) G1_LDY(6)
  G1_STAGE(1)
  G1_LDY(9) G1_LDY(12) G1_LDY(15)
  G1_STAGE(2)
  f32x4 st0, st1;
  {
    const char* sp = ws + OFF_ST + (size_t)(rb * 256 + w * 32) * 64;
    const unsigned so = (lane & 15) * 64 + g * 16;
    GLD16(st0, so, sp, 0); GLD16N(st1, so, sp, 1024);
  }
  const LAS char* wl = (const LAS char*)smem + lane * 16;
  const LAS char* uvl = (const LAS char*)smem + G1_UV + g * 16;
  f32x4 acc0[4], acc1[4];
  float mu0, rs0, mu1, rs1;
  char* hd = ws + OFF_HT + (size_t)(rb * 16 + w * 2) * (576 * 256) + lane * 16;
#define G1_ZERO() _Pragma("unroll") for (int e = 0; e < 4; ++e) { acc0[e] = f32x4{0.f, 0.f, 0.f, 0.f}; acc1[e] = f32x4{0.f, 0.f, 0.f, 0.f}; }
#define G1_KT(slot, i, kt)                                                                              \
  _Pragma("unroll") for (int e = 0; e < 4; ++e) {                                                       \
    const bf16x8 wf = *(const LAS bf16x8*)(wl + ((slot) & 3) * G1_SLOT + (i) * 4096 + e * 1024);        \
    acc0[e] = MFMA16(wf, y0[kt], acc0[e]); acc1[e] = MFMA16(wf, y1[kt], acc1[e]);                       \
  }
#define G1_SLOTMM(slot) _Pragma("unroll") for (int i = 0; i < 9; ++i) { G1_KT(slot, i, ((slot) & 1) * 9 + i) }
#define G1_ACT(A, U, V, mu, rs, a) (fmaxf(rs * (A[a] - mu * U[a]) + V[a], 0.f) * (rs * (A[2 + a] - mu * U[2 + a]) + V[2 + a]))
#define G1_EPI(q)                                                                                       \
  {                                                                                                     \
    float h0[4][2], h1[4][2];                                                                           \
    _Pragma("unroll") for (int e = 0; e < 4; ++e) {                                                     \
      const f32x4 U = *(const LAS f32x4*)(uvl + ((q) * 4 + e) * 128), V = *(const LAS f32x4*)(uvl + ((q) * 4 + e) * 128 + 64); \
      h0[e][0] = G1_ACT(acc0[e], U, V, mu0, rs0, 0); h0[e][1] = G1_ACT(acc0[e], U, V, mu0, rs0, 1);     \
      h1[e][0] = G1_ACT(acc1[e], U, V, mu1, rs1, 0); h1[e][1] = G1_ACT(acc1[e], U, V, mu1, rs1, 1);     \
    }                                                                                                   \
    const u32x4 o0 = {pk2(h0[0][0], h0[1][0]), pk2(h0[2][0], h0[3][0]), pk2(h0[0][1], h0[1][1]), pk2(h0[2][1], h0[3][1])}; \
    const u32x4 o1 = {pk2(h1[0][0], h1[1][0]), pk2(h1[2][0], h1[3][0]), pk2(h1[0][1], h1[1][1]), pk2(h1[2][1], h1[3][1])}; \
    *(u32x4*)(hd + (qfull + (q)) * 1024) = o0;                                                          \
    *(u32x4*)(hd + (qfull + (q)) * 1024 + 576 * 256) = o1;                                              \
    G1_ZERO()                                                                                           \
  }
  G1_ZERO()
  WAITV_BAR(48);
#define G1_FIRST(slot, i, kt, N) asm volatile("s_waitcnt vmcnt(%2)" : "+v"(y0[kt]), "+v"(y1[kt]) : "i"(N) : "memory"); G1_KT(slot, i, kt)
  G1_FIRST(0, 0, 0, 46) G1_FIRST(0, 1, 1, 44) G1_FIRST(0, 2, 2, 42) G1_FIRST(0, 3, 3, 40) G1_FIRST(0, 4, 4, 38)
  G1_FIRST(0, 5, 5, 36) G1_FIRST(0, 6, 6, 34) G1_FIRST(0, 7, 7, 32) G1_FIRST(0, 8, 8, 30)
  WAITV_BAR(25);
  G1_STAGE(3)
  G1_FIRST(1, 0, 9, 28) G1_FIRST(1, 1, 10, 26) G1_FIRST(1, 2, 11, 24) G1_FIRST(1, 3, 12, 22) G1_FIRST(1, 4, 13, 20)
  G1_FIRST(1, 5, 14, 18) G1_FIRST(1, 6, 15, 16) G1_FIRST(1, 7, 16, 14) G1_FIRST(1, 8, 17, 12)
  asm volatile("s_waitcnt vmcnt(5)" : "+v"(st0), "+v"(st1)::"memory");
  {
    float m0 = st0[0] + st0[2], m1 = st1[0] + st1[2];
    m0 += __shfl_xor(m0, 16); m0 += __shfl_xor(m0, 32); m1 += __shfl_xor(m1, 16); m1 += __shfl_xor(m1, 32);
    mu0 = m0 * 0.125f; mu1 = m1 * 0.125f;
    float q0 = st0[1] + st0[3] + 72.f * ((st0[0] - mu0) * (st0[0] - mu0) + (st0[2] - mu0) * (st0[2] - mu0));
    float q1 = st1[1] + st1[3] + 72.f * ((st1[0] - mu1) * (st1[0] - mu1) + (st1[2] - mu1) * (st1[2] - mu1));
    q0 += __shfl_xor(q0, 16); q0 += __shfl_xor(q0, 32); q1 += __shfl_xor(q1, 16); q1 += __shfl_xor(q1, 32);
    rs0 = rsqrtf(q0 * (1.f / D) + LN_EPS); rs1 = rsqrtf(q1 * (1.f / D) + LN_EPS);
  }
  const bool early = w < 4;
#define G1_QUAD_END(q, NEXT_STAGE, Ne, Nl)                                   \
  if (early) { G1_EPI(q) WAITV_BAR(Ne); NEXT_STAGE }                         \
  else { WAITV_BAR(Nl); NEXT_STAGE G1_EPI(q) }
  G1_QUAD_END(0, G1_STAGE(4), 7, 5)
  G1_SLOTMM(2)
  WAITV_BAR(7);
  G1_STAGE(5)
  G1_SLOTMM(3)
  G1_QUAD_END(1, G1_STAGE(6), 7, 5)
  G1_SLOTMM(4)
  WAITV_BAR(7);
  G1_STAGE(7)
  G1_SLOTMM(5)
  G1_QUAD_END(2, G1_STAGE(8), 7, 5)
  G1_SLOTMM(6)
  WAITV_BAR(7);
  G1_SLOTMM(7)
  G1_QUAD_END(3, , 2, 0)
#pragma unroll
  for (int kt = 0; kt < 18; ++kt)
#pragma unroll
    for (int e = 0; e < 2; ++e) {
      const bf16x8 wf = *(const LAS bf16x8*)(wl + kt * 2048 + e * 1024);
      acc0[e] = MFMA16(wf, y0[kt], acc0[e]); acc1[e] = MFMA16(wf, y1[kt], acc1[e]);
    }
  {
    float h0[2][2], h1[2][2];
#pragma unroll
    for (int e = 0; e < 2; ++e) {
      const f32x4 U = *(const LAS f32x4*)(uvl + (16 + e) * 128), V = *(const LAS f32x4*)(uvl + (16 + e) * 128 + 64);
      h0[e][0] = G1_ACT(acc0[e], U, V, mu0, rs0, 0); h0[e][1] = G1_ACT(acc0[e], U, V, mu0, rs0, 1);
      h1[e][0] = G1_ACT(acc1[e], U, V, mu1, rs1, 0); h1[e][1] = G1_ACT(acc1[e], U, V, mu1, rs1, 1);
    }
    char* hp = hd + qhalf * 1024 + odd * 4;
    *(unsigned*)(hp) = pk2(h0[0][0], h0[1][0]); *(unsigned*)(hp + 8) = pk2(h0[0][1], h0[1][1]);
    *(unsigned*)(hp + 576 * 256) = pk2(h1[0][0], h1[1][0]); *(unsigned*)(hp + 576 * 256 + 8) = pk2(h1[0][1], h1[1][1]);
  }
}

constexpr int G2_DEPTH = 6, G2_LDS = 4 * 4 * 5 * 1024;
__global__ __launch_bounds__(256, 1) void k_ffn2(char* __restrict__ ws, u16* __restrict__ out) {
  extern __shared__ __attribute__((aligned(16))) char smem[];
  const int lane = threadIdx.x & 63, w = wave_id(), b = blockIdx.x, g = lane >> 4;
  const int xg = b & 7, j = b >> 3;
  const int tb = (xg >> 1) * 8 + (j >> 2), c = (xg & 1) * 4 + (j & 3);
  const char* hsrc = ws + OFF_HT + (size_t)(tb * 4) * (576 * 256) + w * 1024 + lane * 16;
  const char* wsrc = ws + OFF_W2T + (size_t)c * (144 * 4608) + w * 4608;
  const unsigned o4 = lane * 16, o5 = 4096 + g * 128 + (lane & 7) * 16;
  f32x4 acc[4][5];
#pragma unroll
  for (int t = 0; t < 4; ++t)
#pragma unroll
    for (int e = 0; e < 5; ++e) acc[t][e] = f32x4{0.f, 0.f, 0.f, 0.f};
  bf16x8 hf[G2_DEPTH][4], wf[G2_DEPTH][5];
#define G2_LOAD(s, it)                                                                                     \
  {                                                                                                        \
    _Pragma("unroll") for (int t = 0; t < 4; ++t) hf[s][t] = *(const bf16x8*)(hsrc + (size_t)t * (576 * 256) + (it) * 4096); \
    const char* wp = wsrc + (size_t)(it) * (4 * 4608);                                                     \
    _Pragma("unroll") for (int e = 0; e < 4; ++e) wf[s][e] = *(const bf16x8*)(wp + o4 + e * 1024);         \
    wf[s][4] = *(const bf16x8*)(wp + o5);                                                                  \
  }
#pragma unroll
  for (int s = 0; s < G2_DEPTH; ++s) G2_LOAD(s, s)
  SBAR();
#pragma unroll 1
  for (int it0 = 0; it0 < 36; it0 += G2_DEPTH) {
#pragma unroll
    for (int s = 0; s < G2_DEPTH; ++s) {
#pragma unroll
      for (int t = 0; t < 4; ++t)
#pragma unroll
        for (int e = 0; e < 5; ++e) acc[t][e] = MFMA16(wf[s][e], hf[s][t], acc[t][e]);
      SBAR();
      if (it0 + G2_DEPTH < 36) G2_LOAD(s, it0 + G2_DEPTH + s)
      SBAR();
    }
  }
  LAS char* rl = (LAS char*)smem + lane * 16;
#pragma unroll
  for (int t = 0; t < 4; ++t)
    if (t != w)
#pragma unroll
      for (int e = 0; e < 5; ++e) *(LAS f32x4*)(rl + ((t * 4 + w) * 5 + e) * 1024) = acc[t][e];
  f32x4 r[5];
#pragma unroll
  for (int t = 0; t < 4; ++t)
    if (t == w)
#pragma unroll
      for (int e = 0; e < 5; ++e) r[e] = acc[t][e];
  const int token = tb * 64 + w * 16 + (lane & 15);
  const char* x1p = ws + OFF_X1 + (size_t)(token >> 4) * (72 * 256) + (token & 15) * 16 + 9 * c * 256;
  const u32x4 xa = *(const u32x4*)(x1p + 2 * g * 256), xb = *(const u32x4*)(x1p + (2 * g + 1) * 256);
  const u32x2 xc = *(const u32x2*)(x1p + 8 * 256 + (g & 1) * 8);
  const float* gbp = (const float*)(ws + OFF_MODF) + 5 * D;
  const int d0 = 72 * c + 16 * g, d1 = 72 * c + 64 + 4 * (g & 1);
  f32x4 gb[5];
#pragma unroll
  for (int i = 0; i < 4; ++i) gb[i] = *(const f32x4*)(gbp + d0 + 4 * i);
  gb[4] = *(const f32x4*)(gbp + d1);
  __syncthreads();
#pragma unroll
  for (int s = 1; s < 4; ++s) {
    const int ow = (w + s) & 3;
#pragma unroll
    for (int e = 0; e < 5; ++e) r[e] += *(const LAS f32x4*)(rl + ((w * 4 + ow) * 5 + e) * 1024);
  }
  u32x4 oa, ob;
#pragma unroll
  for (int i = 0; i < 4; ++i) {
    const unsigned w0 = i < 2 ? xa[2 * i] : xb[2 * i - 4], w1 = i < 2 ? xa[2 * i + 1] : xb[2 * i - 3];
    const unsigned r0 = pk2(bf_lo(w0) + gb[i][0] * r[0][i], bf_hi(w0) + gb[i][1] * r[1][i]);
    const unsigned r1 = pk2(bf_lo(w1) + gb[i][2] * r[2][i], bf_hi(w1) + gb[i][3] * r[3][i]);
    if (i < 2) { oa[2 * i] = r0; oa[2 * i + 1] = r1; } else { ob[2 * i - 4] = r0; ob[2 * i - 3] = r1; }
  }
  u16* op = out + (size_t)token * D;
  *(u32x4*)(op + d0) = oa; *(u32x4*)(op + d0 + 8) = ob;
  if (g < 2)
    *(u32x2*)(op + d1) = u32x2{pk2(bf_lo(xc[0]) + gb[4][0] * r[4][0], bf_hi(xc[0]) + gb[4][1] * r[4][1]),
                               pk2(bf_lo(xc[1]) + gb[4][2] * r[4][2], bf_hi(xc[1]) + gb[4][3] * r[4][3])};
}

extern "C" void kernel_launch(void* const* d_in, const int* in_sizes, int n_in, void* d_out, int out_size, void* d_ws, size_t ws_size,
                              hipStream_t stream) {
  const u16* x = (const u16*)d_in[0];
  const u16* wq = (const u16*)d_in[1];
  const u16* wk = (const u16*)d_in[2];
  const u16* wv = (const u16*)d_in[3];
  const u16* wo = (const u16*)d_in[4];
  const u16* wg = (const u16*)d_in[6];
  const u16* wu = (const u16*)d_in[7];
  const u16* wd = (const u16*)d_in[8];
  const u16* cv = (const u16*)d_in[9];
  const u16* wm = (const u16*)d_in[10];
  char* ws = (char*)d_ws;
  (void)hipFuncSetAttribute((const void*)k_qkv, hipFuncAttributeMaxDynamicSharedMemorySize, SG_LDS);
  (void)hipFuncSetAttribute((const void*)k_attn, hipFuncAttributeMaxDynamicSharedMemorySize, ATT_LDS);
  (void)hipFuncSetAttribute((const void*)k_oproj, hipFuncAttributeMaxDynamicSharedMemorySize, SG_LDS);
  (void)hipFuncSetAttribute((const void*)k_ffn1, hipFuncAttributeMaxDynamicSharedMemorySize, G1_LDS);
  (void)hipFuncSetAttribute((const void*)k_ffn2, hipFuncAttributeMaxDynamicSharedMemorySize, G2_LDS);
  k_prep<<<P_GEMV + P_LN + P_WQ + P_WO + P_CONST, 384, 0, stream>>>(x, wq, wk, wv, wo, cv, wm, ws);
  k_qkv<<<256, 512, SG_LDS, stream>>>(ws);
  k_attn<<<256, 1024, ATT_LDS, stream>>>(wg, wu, wd, ws);
  k_oproj<<<256, 512, SG_LDS, stream>>>(x, ws);
  k_ffn1<<<256, 512, G1_LDS, stream>>>(ws);
  k_ffn2<<<256, 256, G2_LDS, stream>>>(ws, (u16*)d_out);
}
```

```cpp
#include <hip/hip_runtime.h>
#include <hip/hip_bf16.h>
#include <stdint.h>

typedef unsigned short u16;
typedef short bf16x8 __attribute__((ext_vector_type(8)));
typedef short bf16x4 __attribute__((ext_vector_type(4)));
typedef float f32x2 __attribute__((ext_vector_type(2)));
typedef float f32x4 __attribute__((ext_vector_type(4)));
typedef float f32x16 __attribute__((ext_vector_type(16)));
typedef unsigned u32x2 __attribute__((ext_vector_type(2)));
typedef unsigned u32x4 __attribute__((ext_vector_type(4)));
typedef __bf16 bf2_t __attribute__((ext_vector_type(2)));
#define LAS __attribute__((address_space(3)))
#define DI __device__ __forceinline__
#define SBAR() __builtin_amdgcn_sched_barrier(0)
#define PIN(x) asm volatile("" : "+v"(x))
#define MFMA16(a, b, c) __builtin_amdgcn_mfma_f32_16x16x32_bf16(a, b, c, 0, 0, 0)
#define MFMA32(a, b, c) __builtin_amdgcn_mfma_f32_32x32x16_bf16(a, b, c, 0, 0, 0)
#define MFMA32K8(a, b, c) __builtin_amdgcn_mfma_f32_32x32x8bf16_1k(a, b, c, 0, 0, 0)

constexpr int S = 2048, D = 576, NH = 32, DH = 18, F = 4608;
constexpr float LN_EPS = 1e-5f;

constexpr size_t ACT_B = (size_t)S * D * 2;
constexpr int KV_TILE = 2496;
constexpr size_t OFF_MODF = 0;
constexpr size_t OFF_MODB = 16384;
constexpr size_t OFF_N1 = 32768;
constexpr size_t OFF_WQT = OFF_N1 + ACT_B;
constexpr size_t WT_B = 8 * 18 * 5 * 1024;
constexpr size_t OFF_WOT = OFF_WQT + WT_B;
constexpr size_t OFF_Q1 = OFF_WOT + WT_B;
constexpr size_t OFF_Q2 = OFF_Q1 + (size_t)NH * S * 32;
constexpr size_t OFF_KV = OFF_Q2 + (size_t)NH * S * 4;
constexpr size_t OFF_OT = OFF_KV + 160 * 1024;
constexpr size_t OFF_X1 = OFF_OT + ACT_B;
constexpr size_t OFF_Y = OFF_X1 + ACT_B;
constexpr size_t OFF_ST = OFF_Y + ACT_B;
constexpr size_t OFF_UVP = OFF_ST + (size_t)S * 64;
constexpr size_t OFF_UV = OFF_UVP + (size_t)18 * 9216 * 8;
constexpr size_t OFF_W1T = OFF_UV + (size_t)9216 * 16;
constexpr size_t OFF_W2T = OFF_W1T + (size_t)144 * 18 * 4096;
constexpr size_t OFF_HT = OFF_W2T + (size_t)8 * 144 * 4608;
constexpr size_t WS_END = OFF_HT + (size_t)S * F * 2;

#define STAMP_DECL
#define STAMP(i)
#define STAMP_FLUSH(kid)

DI float bf_lo(unsigned w) { return __uint_as_float(w << 16); }
DI float bf_hi(unsigned w) { return __uint_as_float(w & 0xffff0000u); }
DI float bf2f(u16 b) { return __uint_as_float((unsigned)b << 16); }
DI unsigned pk2(float lo, float hi) { f32x2 v = {lo, hi}; return __builtin_bit_cast(unsigned, __builtin_convertvector(v, bf2_t)); }
DI float rbf(float x) { return bf_lo(pk2(x, x)); }
DI unsigned plo(unsigned a, unsigned b) { return __builtin_amdgcn_perm(b, a, 0x05040100u); }
DI unsigned phi(unsigned a, unsigned b) { return __builtin_amdgcn_perm(b, a, 0x07060302u); }
DI float RELU(float x) { float r; asm("v_max_f32 %0, 0, %1" : "=v"(r) : "v"(x)); return r; }
DI int wave_id() { return __builtin_amdgcn_readfirstlane(threadIdx.x >> 6); }

template <int N> DI void glds(const void* sbase, unsigned voff, unsigned lds) {
  unsigned keep;
  if constexpr (N == 1)
    asm volatile("s_mov_b32 %0, m0\n\ts_mov_b32 m0, %3\n\ts_nop 2\n\tglobal_load_lds_dwordx4 %1, %2\n\ts_mov_b32 m0, %0"
                 : "=&s"(keep) : "v"(voff), "s"(sbase), "s"(lds) : "memory");
  if constexpr (N == 2)
    asm volatile("s_mov_b32 %0, m0\n\ts_mov_b32 m0, %3\n\ts_nop 2\n\tglobal_load_lds_dwordx4 %1, %2\n\tglobal_load_lds_dwordx4 %1, %2 offset:1024\n\ts_mov_b32 m0, %0"
                 : "=&s"(keep) : "v"(voff), "s"(sbase), "s"(lds) : "memory");
  if constexpr (N == 4)
    asm volatile("s_mov_b32 %0, m0\n\ts_mov_b32 m0, %3\n\ts_nop 2\n\tglobal_load_lds_dwordx4 %1, %2\n\tglobal_load_lds_dwordx4 %1, %2 offset:1024\n\t"
                 "global_load_lds_dwordx4 %1, %2 offset:2048\n\tglobal_load_lds_dwordx4 %1, %2 offset:3072\n\ts_mov_b32 m0, %0"
                 : "=&s"(keep) : "v"(voff), "s"(sbase), "s"(lds) : "memory");
}
DI void glds45(const void* sbase, unsigned voff, unsigned lds) {
  unsigned keep;
  asm volatile("s_mov_b32 %0, m0\n\ts_mov_b32 m0, %3\n\ts_mov_b32 exec_hi, 0\n\ts_nop 1\n\tglobal_load_lds_dwordx4 %1, %2\n\ts_mov_b32 exec_hi, -1\n\t"
               "global_load_lds_dwordx4 %1, %2 offset:512\n\tglobal_load_lds_dwordx4 %1, %2 offset:1536\n\t"
               "global_load_lds_dwordx4 %1, %2 offset:2560\n\tglobal_load_lds_dwordx4 %1, %2 offset:3584\n\ts_mov_b32 m0, %0"
               : "=&s"(keep) : "v"(voff), "s"(sbase), "s"(lds) : "memory");
}
DI void st16_wt(void* p, u32x4 v) { asm volatile("global_store_dwordx4 %0, %1, off sc1\n\ts_nop 1" ::"v"(p), "v"(v) : "memory"); }
DI void st8_wt(void* p, u32x2 v) { asm volatile("global_store_dwordx2 %0, %1, off sc1" ::"v"(p), "v"(v) : "memory"); }
#define AMFMA16(acc, a, b) asm volatile("v_mfma_f32_16x16x32_bf16 %0, %1, %2, %0" : "+v"(acc) : "v"(a), "v"(b))
#define AMFMA16Z(acc, a, b) asm volatile("v_mfma_f32_16x16x32_bf16 %0, %1, %2, 0" : "=&v"(acc) : "v"(a), "v"(b))
#define GLD16(dst, voff, sbase, imm) asm volatile("s_nop 4\n\tglobal_load_dwordx4 %0, %1, %2 offset:%3" : "=v"(dst) : "v"(voff), "s"(sbase), "i"(imm) : "memory")
#define GLD16N(dst, voff, sbase, imm) asm volatile("global_load_dwordx4 %0, %1, %2 offset:%3" : "=v"(dst) : "v"(voff), "s"(sbase), "i"(imm) : "memory")
#define WAITV(n) asm volatile("s_waitcnt vmcnt(%0)" ::"i"(n) : "memory")
#define WAITV_BAR(n) asm volatile("s_waitcnt vmcnt(%0) lgkmcnt(0)\n\ts_barrier" ::"i"(n) : "memory")
#define LDS_BAR() asm volatile("s_waitcnt lgkmcnt(0)\n\ts_barrier" ::: "memory")

DI void quad_transpose(const u32x2 (&L)[8], u32x4 (&T)[4]) {
#pragma unroll
  for (int m = 0; m < 4; ++m) {
    T[0][m] = plo(L[2 * m][0], L[2 * m + 1][0]);
    T[1][m] = phi(L[2 * m][0], L[2 * m + 1][0]);
    T[2][m] = plo(L[2 * m][1], L[2 * m + 1][1]);
    T[3][m] = phi(L[2 * m][1], L[2 * m + 1][1]);
  }
}
DI int ofeat(int P, int j) {
  const int c = P / 9, i = P - 9 * c;
  return i < 8 ? 18 * (4 * c + (i >> 1)) + 8 * (i & 1) + j : 18 * (4 * c + (j >> 1)) + 16 + (j & 1);
}

constexpr int P_GEMV = 216, P_LN = 88, P_WQ = 24, P_WO = 24, P_CONST = 1;
__global__ __launch_bounds__(384) void k_prep(const u16* __restrict__ x, const u16* __restrict__ wq, const u16* __restrict__ wk,
                                             const u16* __restrict__ wv, const u16* __restrict__ wo, const u16* __restrict__ cvec,
                                             const u16* __restrict__ wmod, char* __restrict__ ws) {
  __shared__ float red[6][16];
  const int tid = threadIdx.x, lane = tid & 63, w = tid >> 6;
  int b = blockIdx.x;
  STAMP_DECL; STAMP(0);
  if (b < P_GEMV) {
    const int strip = ((b >> 5) * 8 + (b & 7)) * 4 + ((b >> 3) & 3);
    const int sidx = (b < 192) ? strip : b;
    const int ch = tid & 1, r0 = tid >> 1;
    u32x4 wv4[3]; float sc[3];
#pragma unroll
    for (int p = 0; p < 3; ++p) wv4[p] = *(const u32x4*)(wmod + (size_t)(r0 + 192 * p) * 3456 + sidx * 16 + ch * 8);
#pragma unroll
    for (int p = 0; p < 3; ++p) { const float cv = bf2f(cvec[r0 + 192 * p]); sc[p] = rbf(cv / (1.f + __expf(-cv))); }
    float acc[8];
#pragma unroll
    for (int j = 0; j < 4; ++j) {
      acc[2 * j] = sc[0] * bf_lo(wv4[0][j]) + sc[1] * bf_lo(wv4[1][j]) + sc[2] * bf_lo(wv4[2][j]);
      acc[2 * j + 1] = sc[0] * bf_hi(wv4[0][j]) + sc[1] * bf_hi(wv4[1][j]) + sc[2] * bf_hi(wv4[2][j]);
    }
#pragma unroll
    for (int o = 2; o < 64; o <<= 1)
#pragma unroll
      for (int j = 0; j < 8; ++j) acc[j] += __shfl_xor(acc[j], o);
    if (lane < 2)
#pragma unroll
      for (int j = 0; j < 8; ++j) red[w][lane * 8 + j] = acc[j];
    __syncthreads();
    if (tid < 16) {
      float s = red[0][tid] + red[1][tid] + red[2][tid] + red[3][tid] + red[4][tid] + red[5][tid];
      s = rbf(s);
      const int col = sidx * 16 + tid, which = col / D;
      if (which == 1 || which == 4) s = rbf(1.f + s);
      ((float*)(ws + OFF_MODF))[col] = s;
      ((u16*)(ws + OFF_MODB))[col] = (u16)(__float_as_uint(s) >> 16);
    }
    STAMP(1); STAMP_FLUSH(0);
    return;
  }
  b -= P_GEMV;
  if (b < P_LN) {
    const int wv_ = (b >> 3) * 6 + w;
    if (wv_ >= 64) return;
    const int token = (b & 7) * 256 + wv_ * 4 + (lane >> 4), sub = lane & 15;
    u32x2 xv[9];
#pragma unroll
    for (int i = 0; i < 9; ++i) xv[i] = *(const u32x2*)(x + (size_t)token * D + (sub + 16 * i) * 4);
    float v[36], s = 0.f;
#pragma unroll
    for (int i = 0; i < 9; ++i) { v[4 * i] = bf_lo(xv[i][0]); v[4 * i + 1] = bf_hi(xv[i][0]); v[4 * i + 2] = bf_lo(xv[i][1]); v[4 * i + 3] = bf_hi(xv[i][1]); }
#pragma unroll
    for (int i = 0; i < 36; ++i) s += v[i];
#pragma unroll
    for (int o = 1; o < 16; o <<= 1) s += __shfl_xor(s, o);
    const float mean = s * (1.f / D);
    float q = 0.f;
#pragma unroll
    for (int i = 0; i < 36; ++i) { v[i] -= mean; q += v[i] * v[i]; }
#pragma unroll
    for (int o = 1; o < 16; o <<= 1) q += __shfl_xor(q, o);
    const float rstd = rsqrtf(q * (1.f / D) + LN_EPS);
    char* dst = ws + OFF_N1 + (size_t)(token >> 4) * (72 * 256) + (token & 15) * 16;
#pragma unroll
    for (int i = 0; i < 9; ++i) {
      const int hp = sub + 16 * i;
      u32x2 o2 = {pk2(v[4 * i] * rstd, v[4 * i + 1] * rstd), pk2(v[4 * i + 2] * rstd, v[4 * i + 3] * rstd)};
      *(u32x2*)(dst + (hp >> 1) * 256 + (hp & 1) * 8) = o2;
    }
    STAMP(1); STAMP_FLUSH(0);
    return;
  }
  b -= P_LN;
  const int r = lane & 15, g = lane >> 4;
  if (b < P_WQ + P_WO) {
    const bool isq = b < P_WQ;
    const int unit = (isq ? b : b - P_WQ) * 6 + w;
    const int c = unit & 7, kt = unit >> 3;
    const u16* W = isq ? wq : wo;
    u32x2 L[8]; u16 E[8];
    const int col = isq ? 18 * (4 * c + (r >> 2)) + 4 * (r & 3) : 72 * c + 4 * r;
    const u16* ep = W; int pitch = D; bool ev = false;
    if (r < 8) { ep = W + (isq ? 18 * (4 * c + (r >> 1)) + 16 + (r & 1) : 72 * c + 64 + r); ev = true; }
    else if (isq) {
      const int rr = r - 8;
      if (c < 2) { ep = wk + 8 * c + rr; ev = true; }
      else if (c == 2) { if (rr < 2) { ep = wk + 16 + rr; ev = true; } else if (rr >= 4 && rr < 6) { ep = wv + 12 + rr; ev = true; } }
      else if (c < 5) { ep = wv + 8 * (c - 3) + rr; ev = true; }
      if (ev) pitch = DH;
    }
#pragma unroll
    for (int j = 0; j < 8; ++j) {
      const int k = isq ? 32 * kt + 8 * g + j : ofeat(4 * kt + g, j);
      const unsigned* p = (const unsigned*)(W + (size_t)k * D + col);
      L[j][0] = p[0]; L[j][1] = p[1];
      E[j] = ep[k * pitch];
    }
#pragma unroll
    for (int j = 0; j < 8; ++j) E[j] = ev ? E[j] : (u16)0;
    u32x4 T[4];
    quad_transpose(L, T);
    char* dst = ws + (isq ? OFF_WQT : OFF_WOT) + (size_t)(c * 18 + kt) * 5120 + lane * 16;
#pragma unroll
    for (int e = 0; e < 4; ++e) *(u32x4*)(dst + e * 1024) = T[e];
    u32x4 t4 = {E[0] | ((unsigned)E[1] << 16), E[2] | ((unsigned)E[3] << 16), E[4] | ((unsigned)E[5] << 16), E[6] | ((unsigned)E[7] << 16)};
    *(u32x4*)(dst + 4096) = t4;
    STAMP(1); STAMP_FLUSH(0);
    return;
  }
  if (tid < 256) {
    const int tile = tid >> 2, sh = tid & 3;
    const u32x4 ones = {0x3f803f80u, 0x3f803f80u, 0x3f803f80u, 0x3f803f80u};
    *(u32x4*)(ws + OFF_KV + (size_t)tile * KV_TILE + 1024 + sh * 304 + 18 * 16) = ones;
  }
}

constexpr int SG_LDS = 92160 + 8192;
template <int MOD, class FN>
DI void small_gemm(const char* act, const char* wt, const char* modf, int tb, int c, char* smem, f32x4 (&acc)[5], FN&& mid) {
  const int lane = threadIdx.x & 63, w = wave_id(), tt = w & 3, kh = w >> 2, g = lane >> 4;
  const unsigned voff = lane * 16, lds0 = (unsigned)(uintptr_t)smem;
  const char* wsrc = wt + (size_t)c * 92160;
  const char* asrc = act + (size_t)(tb * 4 + tt) * (72 * 256) + kh * 9216;
  bf16x8 a[9];
  const int pofs = kh * 46080 + (tt == 3 ? 11 : 4 * tt) * 1024;
  glds<1>(modf + w * 1024, voff, lds0 + 92160 + w * 1024);
  glds<4>(wsrc + pofs, voff, lds0 + pofs);
  GLD16(a[0], voff, asrc, 0); GLD16N(a[1], voff, asrc, 1024); GLD16N(a[2], voff, asrc, 2048);
  glds<4>(wsrc + pofs + 15360, voff, lds0 + pofs + 15360);
  { const char* p = asrc + 3072; GLD16(a[3], voff, p, 0); GLD16N(a[4], voff, p, 1024); GLD16N(a[5], voff, p, 2048); }
#pragma unroll
  for (int e = 0; e < 5; ++e) acc[e] = f32x4{0.f, 0.f, 0.f, 0.f};
  const LAS char* wl = (const LAS char*)smem + kh * 46080 + lane * 16;
  const LAS char* ml = (const LAS char*)smem + 92160 + (kh * 288 + g * 8) * 4;
#define SG_STEP(i)                                                                                   \
  {                                                                                                  \
    bf16x8 bfr = a[i];                                                                               \
    if (MOD == 1) {                                                                                  \
      const f32x4 s0 = *(const LAS f32x4*)(ml + (i) * 128), s1 = *(const LAS f32x4*)(ml + (i) * 128 + 16);                 \
      const f32x4 c0 = *(const LAS f32x4*)(ml + 2304 + (i) * 128), c1 = *(const LAS f32x4*)(ml + 2304 + (i) * 128 + 16);   \
      const u32x4 n = __builtin_bit_cast(u32x4, bfr);                                                \
      u32x4 h;                                                                                       \
      h[0] = pk2(bf_lo(n[0]) * c0[0] + s0[0], bf_hi(n[0]) * c0[1] + s0[1]);                          \
      h[1] = pk2(bf_lo(n[1]) * c0[2] + s0[2], bf_hi(n[1]) * c0[3] + s0[3]);                          \
      h[2] = pk2(bf_lo(n[2]) * c1[0] + s1[0], bf_hi(n[2]) * c1[1] + s1[1]);                          \
      h[3] = pk2(bf_lo(n[3]) * c1[2] + s1[2], bf_hi(n[3]) * c1[3] + s1[3]);                          \
      bfr = __builtin_bit_cast(bf16x8, h);                                                           \
    }                                                                                                \
    _Pragma("unroll") for (int e = 0; e < 5; ++e)                                                    \
      acc[e] = MFMA16(*(const LAS bf16x8*)(wl + (i) * 5120 + e * 1024), bfr, acc[e]);                \
  }
  asm volatile("s_waitcnt vmcnt(7)\n\ts_barrier" : "+v"(a[0]), "+v"(a[1]), "+v"(a[2])::"memory");
  SG_STEP(0)
  SBAR();
  glds<4>(wsrc + pofs + 30720, voff, lds0 + pofs + 30720);
  SG_STEP(1)
  SBAR();
  { const char* p = asrc + 6144; GLD16(a[6], voff, p, 0); GLD16N(a[7], voff, p, 1024); GLD16N(a[8], voff, p, 2048); }
  SG_STEP(2)
  asm volatile("s_waitcnt vmcnt(7)\n\ts_barrier" : "+v"(a[3]), "+v"(a[4]), "+v"(a[5])::"memory");
  SG_STEP(3) SG_STEP(4) SG_STEP(5)
  asm volatile("s_waitcnt vmcnt(0)\n\ts_barrier" : "+v"(a[6]), "+v"(a[7]), "+v"(a[8])::"memory");
  SG_STEP(6) SG_STEP(7) SG_STEP(8)
#undef SG_STEP
  LAS char* rl = (LAS char*)smem + (tt & 1) * 5120 + (tt >> 1) * 46080 + lane * 16;
  if (kh == 1)
#pragma unroll
    for (int e = 0; e < 5; ++e) *(LAS f32x4*)(rl + e * 1024) = acc[e];
  LDS_BAR();
  if (kh == 0)
#pragma unroll
    for (int e = 0; e < 5; ++e) acc[e] += *(const LAS f32x4*)(rl + e * 1024);
}

__global__ __launch_bounds__(512) void k_qkv(char* __restrict__ ws) {
  extern __shared__ __attribute__((aligned(16))) char smem[];
  const int b = blockIdx.x, c = b >> 5, tb = (b & 7) * 4 + ((b >> 3) & 3);
  f32x4 acc[5];
  STAMP_DECL; STAMP(0);
  small_gemm<1>(ws + OFF_N1, ws + OFF_WQT, ws + OFF_MODF, tb, c, smem, acc, [&]() {});
  STAMP(1);
  const int lane = threadIdx.x & 63, w = threadIdx.x >> 6;
  if (w >= 4) return;
  const int g = lane >> 4, token = tb * 64 + w * 16 + (lane & 15);
  constexpr float QS = 0.23570226039551584f * 1.4426950408889634f;
  u32x4 q0, q1;
  q0[0] = pk2(acc[0][0] * QS, acc[1][0] * QS); q0[1] = pk2(acc[2][0] * QS, acc[3][0] * QS);
  q0[2] = pk2(acc[0][1] * QS, acc[1][1] * QS); q0[3] = pk2(acc[2][1] * QS, acc[3][1] * QS);
  q1[0] = pk2(acc[0][2] * QS, acc[1][2] * QS); q1[1] = pk2(acc[2][2] * QS, acc[3][2] * QS);
  q1[2] = pk2(acc[0][3] * QS, acc[1][3] * QS); q1[3] = pk2(acc[2][3] * QS, acc[3][3] * QS);
  char* qd = ws + OFF_Q1 + ((size_t)(4 * c + g) * S + token) * 32;
  *(u32x4*)qd = q0; *(u32x4*)(qd + 16) = q1;
  const f32x4 t = acc[4];
  if (g < 2) {
    unsigned* q2 = (unsigned*)(ws + OFF_Q2);
    q2[(size_t)(4 * c + 2 * g) * S + token] = pk2(t[0] * QS, t[1] * QS);
    q2[(size_t)(4 * c + 2 * g + 1) * S + token] = pk2(t[2] * QS, t[3] * QS);
  } else {
    char* kv = ws + OFF_KV + (size_t)(token >> 5) * KV_TILE;
    const int key = token & 31, k16 = token & 15;
    char* vrow = kv + 1024 + (key >> 4) * 608 + ((k16 >> 2) & 1) * 304 + ((k16 & 3) + 4 * (k16 >> 3)) * 2;
    const unsigned p0 = pk2(t[0], t[1]), p1 = pk2(t[2], t[3]);
    if (c < 2) {
      *(u32x2*)(kv + (key + 32 * c) * 16 + (g - 2) * 8) = u32x2{p0, p1};
    } else if (c == 2) {
      if (g == 2) *(u32x2*)(kv + 2240 + key * 8) = u32x2{p0, 0x00003f80u};
      else { *(u16*)(vrow + 16 * 16) = (u16)p0; *(u16*)(vrow + 17 * 16) = (u16)(p0 >> 16); }
    } else if (c < 5) {
      char* vr = vrow + (4 * (c - 3) + 8 * (g - 2)) * 16;
      *(u16*)(vr) = (u16)p0; *(u16*)(vr + 16) = (u16)(p0 >> 16); *(u16*)(vr + 32) = (u16)p1; *(u16*)(vr + 48) = (u16)(p1 >> 16);
    }
  }
  STAMP(2); STAMP_FLUSH(1);
}

constexpr int ATT_LDS = 64 * KV_TILE;
DI int w1_slot(int T) {
  const int t0 = (T / 6) * 6, i = T - t0;
  return t0 + ((t0 & 2) ? (i < 2 ? i + 4 : i - 2) : i);
}
DI void w1_dest(unsigned s, unsigned kt, unsigned& lo, unsigned& hi) {
  const unsigned m = s / 3, i = s - 3 * m, base = 216 * m + 6 * kt;
  lo = (base + (i == 0 ? 0 : (i == 1 ? 4 : 108))) * 1024;
  hi = (base + (i == 0 ? 2 : (i == 1 ? 112 : 110))) * 1024;
}
DI void side_unit(unsigned did, int lane, const u16* wg, const u16* wu, const u16* wd, char* ws) {
  const int r16 = lane & 15, g = lane >> 4;
  const char* src; char* dst;
  unsigned pitch, d2, es, o0, o1, o2, o3;
  const bool isw1 = did < 1296;
  unsigned kt, sp = 0;
  if (isw1) {
    sp = did / 18; kt = did - 18 * sp;
    src = (const char*)((((r16 >> 1) & 1) ? wu : wg) + (size_t)(32 * kt + 8 * g) * F + 64 * sp + 4 * ((r16 >> 2) * 2 + (r16 & 1)));
    pitch = F * 2; d2 = 64; es = 1024;
    dst = ws + OFF_W1T + lane * 16;
    w1_dest(2 * sp, kt, o0, o1); w1_dest(2 * sp + 1, kt, o2, o3);
  } else if (did < 1872) {
    const unsigned id = did - 1296, cc = (id & 3) * 2; kt = id >> 2;
    src = (const char*)(wd + (size_t)(32 * kt + 8 * g) * D + 72 * cc + 4 * r16);
    pitch = D * 2; d2 = 144; es = 1024;
    dst = ws + OFF_W2T + lane * 16;
    o0 = (cc * 144 + kt) * 4608; o1 = o0 + 2048; o2 = o0 + 144 * 4608; o3 = o2 + 2048;
  } else {
    kt = (did - 1872) * 2;
    src = (const char*)(wd + (size_t)(32 * kt + 8 * g) * D + 72 * (r16 >> 1) + 64 + 4 * (r16 & 1));
    pitch = D * 2; d2 = 32 * D * 2; es = 16;
    dst = ws + OFF_W2T + (size_t)(r16 >> 1) * (144 * 4608) + 4096 + g * 128 + (r16 & 1) * 64;
    o0 = kt * 4608; o1 = o0 + 32; o2 = o0 + 4608; o3 = o2 + 32;
  }
  u32x2 La[8], Lb[8];
#pragma unroll
  for (int j = 0; j < 8; ++j) {
    const char* p = src + (isw1 ? j : 4 * (j & 1) + (j >> 1)) * pitch;
    La[j] = *(const u32x2*)p; Lb[j] = *(const u32x2*)(p + d2);
  }
  u32x4 mv = {0u, 0u, 0u, 0u};
  if (isw1 && (r16 & 3) < 2) mv = *(const u32x4*)(ws + OFF_MODB + (((r16 & 1) ? 3 : 4) * D + 32 * kt + 8 * g) * 2);
  u32x4 Ta[4], Tb[4];
  quad_transpose(La, Ta); quad_transpose(Lb, Tb);
  st16_wt(dst + o0, Ta[0]); st16_wt(dst + o0 + es, Ta[1]); st16_wt(dst + o1, Ta[2]); st16_wt(dst + o1 + es, Ta[3]);
  st16_wt(dst + o2, Tb[0]); st16_wt(dst + o2 + es, Tb[1]); st16_wt(dst + o3, Tb[2]); st16_wt(dst + o3 + es, Tb[3]);
  if (isw1) {
    f32x4 da[4], db[4];
#pragma unroll
    for (int e = 0; e < 4; ++e) {
      da[e] = MFMA16(__builtin_bit_cast(bf16x8, mv), __builtin_bit_cast(bf16x8, Ta[e]), (f32x4{0.f, 0.f, 0.f, 0.f}));
      db[e] = MFMA16(__builtin_bit_cast(bf16x8, mv), __builtin_bit_cast(bf16x8, Tb[e]), (f32x4{0.f, 0.f, 0.f, 0.f}));
    }
    f32x2 ua = {da[0][0], da[0][1]}, ub = {db[0][0], db[0][1]};
#pragma unroll
    for (int e = 1; e < 4; ++e)
      if (g == e) { ua = f32x2{da[e][0], da[e][1]}; ub = f32x2{db[e][0], db[e][1]}; }
    f32x2* up = (f32x2*)(ws + OFF_UVP) + (kt * 9216 + sp * 128 + lane);
    up[0] = ua; up[64] = ub;
  }
}

__global__ __launch_bounds__(768, 3) void k_attn(const u16* __restrict__ wg, const u16* __restrict__ wu, const u16* __restrict__ wd,
                                                char* __restrict__ ws) {
  extern __shared__ __attribute__((aligned(16))) char smem[];
  const int lane = threadIdx.x & 63, w = wave_id(), b = blockIdx.x, c = b >> 5, tb = (b & 7) * 4 + ((b >> 3) & 3);
  STAMP_DECL; STAMP(0);
  if (w >= 8) {
    const int lw = w - 8;
    const unsigned voff = lane * 16, lds0 = (unsigned)(uintptr_t)smem;
    const char* kvg = ws + OFF_KV;
    __builtin_amdgcn_s_setprio(3);
    glds<4>(kvg + lw * 4096, voff, lds0 + lw * 4096);
    glds<4>(kvg + 16384 + lw * 4096, voff, lds0 + 16384 + lw * 4096);
    glds<4>(kvg + 32768 + lw * 4096, voff, lds0 + 32768 + lw * 4096);
    asm volatile("s_waitcnt vmcnt(4)\n\ts_barrier" ::: "memory");
#pragma unroll 1
    for (int p = 12 + lw; p < 39; p += 4) glds<4>(kvg + p * 4096, voff, lds0 + p * 4096);
    asm volatile("s_waitcnt vmcnt(0)\n\ts_barrier" ::: "memory");
    STAMP(1);
#pragma unroll 1
    for (unsigned did = b * 4 + lw; did < 1944; did += 1024) side_unit(did, lane, wg, wu, wd, ws);
    STAMP(2); STAMP_FLUSH(6);
    return;
  }
  const int r = lane & 31, h = lane >> 5, th = w >> 2, kq = w & 3;
  bf16x8 q1[4]; u32x2 q2[4];
  {
    const char* qp = ws + OFF_Q1 + ((size_t)(4 * c) * S + tb * 64 + th * 32 + r) * 32 + h * 16;
    const char* qp2 = ws + OFF_Q2 + ((size_t)(4 * c) * S + tb * 64 + th * 32 + r) * 4;
#pragma unroll
    for (int i = 0; i < 4; ++i) {
      q1[i] = *(const bf16x8*)(qp + i * (S * 32));
      const unsigned t = *(const unsigned*)(qp2 + i * (S * 4));
      q2[i] = u32x2{h ? 0u : t, 0u};
    }
  }
  constexpr int TS = 4 * KV_TILE;
  const LAS char* kp = (const LAS char*)smem + kq * KV_TILE + lane * 16;
  const LAS char* vp = (const LAS char*)smem + kq * KV_TILE + 1024 + h * 304 + (r < 18 ? r : 18) * 16;
  const LAS char* k2p = (const LAS char*)smem + kq * KV_TILE + 2240 + r * 8;
  const f32x16 zero16 = {};
  f32x16 sA, sB, o[4];
  u32x4 p0, p1 = {};
  bf16x8 ka, v0, v1 = {};
  bf16x4 kb;
  asm volatile("s_barrier" ::: "memory");
  STAMP(1);
  ka = *(const LAS bf16x8*)kp; kb = *(const LAS bf16x4*)k2p;
#define Q2(i) __builtin_bit_cast(bf16x4, q2[i])
#pragma unroll
  for (int i = 0; i < 4; ++i) { o[i] = MFMA32K8(kb, Q2(i), zero16); o[i] = MFMA32(ka, q1[i], o[i]); }
#pragma unroll
  for (int i = 0; i < 4; ++i) {
    float m = __builtin_fmaxf(o[i][0], o[i][1]);
#pragma unroll
    for (int j = 2; j < 16; j += 2) m = __builtin_fmaxf(__builtin_fmaxf(m, o[i][j]), o[i][j + 1]);
    const auto sw = __builtin_amdgcn_permlane32_swap(__float_as_uint(m), __float_as_uint(m), false, false);
    m = __builtin_fmaxf(__uint_as_float(sw[0]), __uint_as_float(sw[1]));
    const unsigned nm = pk2(-64.f - m, 0.f);
    q2[i][1] = h ? nm << 16 : nm;
    o[i] = zero16;
  }
  sA = MFMA32K8(kb, Q2(0), zero16); sA = MFMA32(ka, q1[0], sA);
#define EX(x) x = __builtin_amdgcn_exp2f(x)
#define ATT_STEP(C, N, i)                                                                                 \
  {                                                                                                       \
    constexpr int ip = ((i) + 3) & 3, in = ((i) + 1) & 3;                                                 \
    if ((i) == 0) v0 = *(const LAS bf16x8*)(vp);                                                          \
    SBAR();                                                                                               \
    EX(C[0]); EX(C[1]); PIN(C); SBAR();                                                                   \
    o[ip] = MFMA32(v1, __builtin_bit_cast(bf16x8, p1), o[ip]);                                            \
    SBAR();                                                                                               \
    if ((i) == 0) v1 = *(const LAS bf16x8*)(vp + 608);                                                    \
    if ((i) == 1) { kp += adv; vp += adv; k2p += adv; }                                                   \
    EX(C[2]); EX(C[3]); p0[0] = pk2(C[0], C[1]); EX(C[4]); p0[1] = pk2(C[2], C[3]); EX(C[5]);             \
    PIN(p0); PIN(C); SBAR();                                                                              \
    N = MFMA32K8(kb, Q2(in), zero16);                                                                     \
    SBAR();                                                                                               \
    if ((i) == 2) kb = *(const LAS bf16x4*)(k2p);                                                         \
    EX(C[6]); EX(C[7]); p0[2] = pk2(C[4], C[5]); EX(C[8]); p0[3] = pk2(C[6], C[7]); EX(C[9]);             \
    PIN(p0); PIN(C); SBAR();                                                                              \
    N = MFMA32(ka, q1[in], N);                                                                            \
    SBAR();                                                                                               \
    if ((i) == 2) ka = *(const LAS bf16x8*)(kp);                                                          \
    EX(C[10]); EX(C[11]); p1[0] = pk2(C[8], C[9]); EX(C[12]); p1[1] = pk2(C[10], C[11]); EX(C[13]);       \
    PIN(p1); PIN(C); SBAR();                                                                              \
    o[i] = MFMA32(v0, __builtin_bit_cast(bf16x8, p0), o[i]);                                              \
    SBAR();                                                                                               \
    EX(C[14]); EX(C[15]); p1[2] = pk2(C[12], C[13]); p1[3] = pk2(C[14], C[15]);                           \
    PIN(p1); SBAR();                                                                                      \
  }
  __builtin_amdgcn_s_setprio(2);
#pragma unroll 1
  for (int it = 0; it < 16; ++it) {
    const int adv = it == 15 ? -15 * TS : TS;
    ATT_STEP(sA, sB, 0) ATT_STEP(sB, sA, 1) ATT_STEP(sA, sB, 2) ATT_STEP(sB, sA, 3)
    if (__builtin_expect((0x4102 >> it) & 1, 0)) {
      if (it == 1) asm volatile("s_barrier" ::: "memory");
      else if (it == 8) __builtin_amdgcn_s_setprio(1);
      else __builtin_amdgcn_s_setprio(0);
    }
  }
  o[3] = MFMA32(v1, __builtin_bit_cast(bf16x8, p1), o[3]);
#undef ATT_STEP
  STAMP(2);
  float ms[4];
#pragma unroll
  for (int i = 0; i < 4; ++i) ms[i] = __uint_as_float(h ? q2[i][1] : q2[i][1] << 16);
  {
    const float lmin = __builtin_fminf(__builtin_fminf(o[0][10], o[1][10]), __builtin_fminf(o[2][10], o[3][10]));
    const float lsum = (o[0][10] + o[1][10]) + (o[2][10] + o[3][10]);
    const bool ok = (lmin >= 0x1p-100f) && (lsum <= 0x1p40f);
    if (__builtin_expect(!__all(ok), 0)) {
#pragma unroll
      for (int i = 0; i < 4; ++i) { o[i] = zero16; ms[i] = INFINITY; q2[i][1] = 0u; }
#pragma unroll 1
      for (int t = 0; t < 16; ++t) {
        const bf16x8 fka = *(const LAS bf16x8*)(kp + t * TS), fv0 = *(const LAS bf16x8*)(vp + t * TS), fv1 = *(const LAS bf16x8*)(vp + t * TS + 608);
        const bf16x4 fkb = *(const LAS bf16x4*)(k2p + t * TS);
#pragma unroll
        for (int i = 0; i < 4; ++i) {
          sA = MFMA32K8(fkb, Q2(i), zero16); sA = MFMA32(fka, q1[i], sA);
          float mt = -ms[i];
#pragma unroll
          for (int j = 0; j < 16; ++j) mt = fmaxf(mt, sA[j]);
          mt = fmaxf(mt, __shfl_xor(mt, 32));
          const float al = (-ms[i] == mt) ? 1.f : __builtin_amdgcn_exp2f(-ms[i] - mt);
          ms[i] = -mt;
#pragma unroll
          for (int j = 0; j < 16; ++j) { o[i][j] *= al; sA[j] = __builtin_amdgcn_exp2f(sA[j] - mt); }
#pragma unroll
          for (int j = 0; j < 4; ++j) { p0[j] = pk2(sA[2 * j], sA[2 * j + 1]); p1[j] = pk2(sA[8 + 2 * j], sA[9 + 2 * j]); }
          o[i] = MFMA32(fv0, __builtin_bit_cast(bf16x8, p0), o[i]);
          o[i] = MFMA32(fv1, __builtin_bit_cast(bf16x8, p1), o[i]);
          SBAR();
        }
      }
    }
  }
#undef EX
#undef Q2
  LDS_BAR();
  STAMP(3);
  LAS char* cl = (LAS char*)smem + th * 49152 + lane * 16;
#pragma unroll
  for (int i = 0; i < 4; ++i) {
    LAS char* d = cl + (i * 4 + kq) * 3072;
    *(LAS f32x4*)(d) = f32x4{o[i][0], o[i][1], o[i][2], o[i][3]};
    *(LAS f32x4*)(d + 1024) = f32x4{o[i][4], o[i][5], o[i][6], o[i][7]};
    *(LAS f32x4*)(d + 2048) = f32x4{o[i][8], o[i][9], o[i][10], ms[i]};
  }
  const int token = tb * 64 + th * 32 + r;
  char* od = ws + OFF_OT + (size_t)(token >> 4) * (72 * 256) + (token & 15) * 16 + 9 * c * 256;
  LDS_BAR();
  {
    f32x4 a0[4], a1[4], a2[4];
#pragma unroll
    for (int j = 0; j < 4; ++j) a2[j] = *(const LAS f32x4*)(cl + (kq * 4 + j) * 3072 + 2048);
#pragma unroll
    for (int j = 0; j < 4; ++j) { a0[j] = *(const LAS f32x4*)(cl + (kq * 4 + j) * 3072); a1[j] = *(const LAS f32x4*)(cl + (kq * 4 + j) * 3072 + 1024); }
    const float mm = __builtin_fminf(__builtin_fminf(a2[0][3], a2[1][3]), __builtin_fminf(a2[2][3], a2[3][3]));
    float f[4], l = 0.f;
#pragma unroll
    for (int j = 0; j < 4; ++j) { f[j] = (a2[j][3] == mm) ? 1.f : __builtin_amdgcn_exp2f(mm - a2[j][3]); l += f[j] * a2[j][2]; }
    const float rl = __builtin_amdgcn_rcpf(l);
    f32x4 r0 = {}, r1 = {};
    float r20 = 0.f, r21 = 0.f;
#pragma unroll
    for (int j = 0; j < 4; ++j) { const float s = f[j] * rl; r0 += a0[j] * s; r1 += a1[j] * s; r20 += a2[j][0] * s; r21 += a2[j][1] * s; }
    *(u32x4*)(od + (2 * kq + h) * 256) = u32x4{pk2(r0[0], r0[1]), pk2(r0[2], r0[3]), pk2(r1[0], r1[1]), pk2(r1[2], r1[3])};
    if (!h) *(unsigned*)(od + 8 * 256 + kq * 4) = pk2(r20, r21);
  }
  STAMP(4); STAMP_FLUSH(2);
}

__global__ __launch_bounds__(512) void k_oproj(const u16* __restrict__ x, char* __restrict__ ws) {
  extern __shared__ __attribute__((aligned(16))) char smem[];
  const int b = blockIdx.x;
  const int c = b >> 5, tb = (b & 7) * 4 + ((b >> 3) & 3);
  const int lane = threadIdx.x & 63, w = wave_id();
  const int g = lane >> 4, token = tb * 64 + (w & 3) * 16 + (lane & 15);
  const int d0 = 72 * c + 16 * g, d1 = 72 * c + 64 + 4 * (g & 1);
  f32x4 acc[5];
  STAMP_DECL; STAMP(0);
  const u32x4 xa = *(const u32x4*)(x + (size_t)token * D + d0), xb = *(const u32x4*)(x + (size_t)token * D + d0 + 8);
  const u32x2 xc = *(const u32x2*)(x + (size_t)token * D + d1);
  f32x2 up[3];
  const int ur = lane % 9, uk = lane / 9, un = b * 36 + (w & 3) * 9 + ur;
  if (w >= 4) {
    const f32x2* p = (const f32x2*)(ws + OFF_UVP) + (size_t)(uk < 6 ? uk : 0) * (3 * 9216) + un;
    up[0] = p[0]; up[1] = p[9216]; up[2] = p[2 * 9216];
  }
  small_gemm<2>(ws + OFF_OT, ws + OFF_WOT, ws + OFF_MODF + 2 * D * 4, tb, c, smem, acc, [&]() {});
  STAMP(1);
  if (w >= 4) {
    f32x2 s = up[0] + up[1] + up[2];
    f32x2 t = s;
#pragma unroll
    for (int k = 1; k < 6; ++k) { t[0] += __shfl(s[0], ur + 9 * k); t[1] += __shfl(s[1], ur + 9 * k); }
    const float uh = rbf(t[0]), vh = rbf(t[1]);
    if (lane < 9) *(u32x4*)(ws + OFF_UV + (w1_slot(un >> 4) * 16 + (un & 15)) * 16) = u32x4{pk2(uh, t[0] - uh), pk2(uh, vh), pk2(t[1] - vh, vh), 0u};
    return;
  }
  const LAS float* ml = (const LAS float*)((const LAS char*)smem + 92160);
  unsigned xo[10], yo[10];
  float s = 0.f, q = 0.f;
#pragma unroll
  for (int i = 0; i < 5; ++i) {
    const f32x4 ga = *(const LAS f32x4*)(ml + (i < 4 ? d0 + 4 * i : d1)), cb = *(const LAS f32x4*)(ml + 2 * D + (i < 4 ? d0 + 4 * i : d1));
    const unsigned w0 = i < 2 ? xa[2 * i] : (i < 4 ? xb[2 * i - 4] : xc[0]), w1 = i < 2 ? xa[2 * i + 1] : (i < 4 ? xb[2 * i - 3] : xc[1]);
    const f32x4 a4 = i < 4 ? f32x4{acc[0][i], acc[1][i], acc[2][i], acc[3][i]} : acc[4];
    const unsigned r0 = pk2(bf_lo(w0) + ga[0] * a4[0], bf_hi(w0) + ga[1] * a4[1]), r1 = pk2(bf_lo(w1) + ga[2] * a4[2], bf_hi(w1) + ga[3] * a4[3]);
    const float v0 = bf_lo(r0), v1 = bf_hi(r0), v2 = bf_lo(r1), v3 = bf_hi(r1);
    xo[2 * i] = r0; xo[2 * i + 1] = r1;
    yo[2 * i] = pk2(v0 * cb[0], v1 * cb[1]); yo[2 * i + 1] = pk2(v2 * cb[2], v3 * cb[3]);
    const float ps = (v0 + v1) + (v2 + v3), pq = v0 * v0 + v1 * v1 + v2 * v2 + v3 * v3;
    if (i < 4 || g < 2) { s += ps; q += pq; }
  }
  const size_t base = (size_t)(token >> 4) * (72 * 256) + (token & 15) * 16 + 9 * c * 256;
  char* x1d = ws + OFF_X1 + base;
  char* yd = ws + OFF_Y + base;
  *(u32x4*)(yd + (2 * g) * 256) = u32x4{yo[0], yo[1], yo[2], yo[3]};
  *(u32x4*)(yd + (2 * g + 1) * 256) = u32x4{yo[4], yo[5], yo[6], yo[7]};
  *(u32x4*)(x1d + (2 * g) * 256) = u32x4{xo[0], xo[1], xo[2], xo[3]};
  *(u32x4*)(x1d + (2 * g + 1) * 256) = u32x4{xo[4], xo[5], xo[6], xo[7]};
  if (g < 2) {
    *(u32x2*)(yd + 8 * 256 + g * 8) = u32x2{yo[8], yo[9]};
    *(u32x2*)(x1d + 8 * 256 + g * 8) = u32x2{xo[8], xo[9]};
  }
  s += __shfl_xor(s, 16); q += __shfl_xor(q, 16);
  s += __shfl_xor(s, 32); q += __shfl_xor(q, 32);
  if (g == 0) *(f32x2*)(ws + OFF_ST + ((size_t)token * 8 + c) * 8) = f32x2{s, q};
  STAMP(2); STAMP_FLUSH(3);
}

DI void ln_stats(const f32x4& st, float& mu, float& sd2) {
  float s = st[0] + st[2], q = st[1] + st[3];
  s += __shfl_xor(s, 16); q += __shfl_xor(q, 16);
  s += __shfl_xor(s, 32); q += __shfl_xor(q, 32);
  mu = s * (1.f / D);
  sd2 = fmaxf(q * (1.f / D) - mu * mu, 0.f) + LN_EPS;
}

#define H_ST16(p, v) st16_wt(p, v)
DI bf16x8 corr_operand(float a, float b, int g) {
  const float ah = rbf(a), bh = rbf(b);
  u32x4 v = {pk2(ah, ah), pk2(a - ah, bh), pk2(bh, b - bh), 0u};
  if (g) v = u32x4{0u, 0u, 0u, 0u};
  return __builtin_bit_cast(bf16x8, v);
}
constexpr int G1_SLOT = 36864, G1_UV = 3 * G1_SLOT, G1_LDS = G1_UV + 5120;
__global__ __launch_bounds__(512, 2) void k_ffn1(char* __restrict__ ws) {
  extern __shared__ __attribute__((aligned(16))) char smem[];
  STAMP_DECL; STAMP(0);
  const int lane = threadIdx.x & 63, w = wave_id(), b = blockIdx.x, g = lane >> 4;
  const int xg = b & 7, jj = b >> 3;
  const int rb = (xg & 1) * 4 + (jj & 3), n = (xg >> 1) * 8 + (jj >> 2);
  const unsigned voff = lane * 16, lds0 = (unsigned)(uintptr_t)smem;
  const char* wsrc = ws + OFF_W1T + (size_t)n * (9 * G1_SLOT) + w * 4608;
  const unsigned ldw = lds0 + w * 4608;
  if (w < 4) __builtin_amdgcn_s_setprio(1);
#define G1_STAGE(src, ring) glds45(src, voff, ldw + (ring) * G1_SLOT);
  const char* ya = ws + OFF_Y + (size_t)(rb * 16 + w * 2) * (72 * 256);
  const char* yb = ya + 72 * 256;
  bf16x8 y0[18], y1[18];
  f32x4 st0, st1;
  asm volatile("s_nop 4" : "+s"(ya), "+s"(yb));
#define G1_LDY(kt) { const unsigned o_ = voff + ((kt) >> 2) * 4096; GLD16N(y0[kt], o_, ya, ((kt) & 3) * 1024); GLD16N(y1[kt], o_, yb, ((kt) & 3) * 1024); }
  G1_STAGE(wsrc, 0)
  G1_LDY(0) G1_LDY(1) G1_LDY(2)
  const LAS char* wl0 = (const LAS char*)smem + lane * 16;
  const LAS char* wl1 = wl0 + 55296;
  asm volatile("" : "+v"(wl1));
  const LAS char* uvl = (const LAS char*)smem + G1_UV + (lane & 15) * 16;
  f32x4 acc0[6], acc1[6];
  bf16x8 fa[6];
  bf16x8 cr0, cr1;
  char* hd = ws + OFF_HT + (size_t)(rb * 16 + w * 2) * (576 * 256) + lane * 16;
#define G1_RD(kt, e) (*(const LAS bf16x8*)(((kt) < 9 ? wl0 : wl1) + ((kt) % 9) * 6144 + (e) * 1024))
#define G1_STEP(C, N, kt)                                                                               \
  _Pragma("unroll") for (int e = 0; e < 6; ++e) {                                                       \
    if ((kt) == 0) { AMFMA16Z(acc0[e], C[e], y0[kt]); AMFMA16Z(acc1[e], C[e], y1[kt]); }                \
    else { AMFMA16(acc0[e], C[e], y0[kt]); AMFMA16(acc1[e], C[e], y1[kt]); }                            \
    SBAR();                                                                                             \
    N[e] = G1_RD(((kt) + 1) % 18, e);                                                                   \
    SBAR();                                                                                             \
  }
#define G1_S(kt) G1_STEP(fa, fa, kt)
#define G1_EPI(sw)                                                                                      \
  {                                                                                                     \
    _Pragma("unroll") for (int e = 0; e < 6; ++e) {                                                     \
      const bf16x8 uf = *(const LAS bf16x8*)(uvl + (sw) * 1536 + e * 256);                              \
      AMFMA16(acc0[e], uf, cr0); AMFMA16(acc1[e], uf, cr1);                                             \
    }                                                                                                   \
    const int t0 = 18 * n + 6 * (sw), hi = (t0 >> 1) & 1;                                               \
    char* pf = hd + ((t0 >> 2) + hi) * 1024;                                                            \
    char* ph = hd + ((t0 >> 2) + 1 - hi) * 1024 + hi * 8;                                               \
    asm volatile("s_nop 7" : "+v"(acc0[4]), "+v"(acc1[4]), "+v"(acc0[5]), "+v"(acc1[5]), "+v"(acc0[3]), "+v"(acc1[3]), "+v"(acc0[2]), "+v"(acc1[2]), \
                 "+v"(acc0[1]), "+v"(acc1[1]), "+v"(acc0[0]), "+v"(acc1[0]));                           \
    unsigned h0[6], h1[6];                                                                              \
    _Pragma("unroll") for (int e = 0; e < 6; ++e) {                                                     \
      h0[e] = pk2(RELU(acc0[e][0]) * acc0[e][2], RELU(acc0[e][1]) * acc0[e][3]);                        \
      h1[e] = pk2(RELU(acc1[e][0]) * acc1[e][2], RELU(acc1[e][1]) * acc1[e][3]);                        \
    }                                                                                                   \
    H_ST16(pf, (u32x4{h0[0], h0[1], h0[2], h0[3]}));                                                    \
    H_ST16(pf + 576 * 256, (u32x4{h1[0], h1[1], h1[2], h1[3]}));                                        \
    *(u32x2*)(ph) = u32x2{h0[4], h0[5]};                                                                \
    *(u32x2*)(ph + 576 * 256) = u32x2{h1[4], h1[5]};                                                    \
    SBAR();                                                                                             \
  }
#define G1_BAR(N) asm volatile("s_waitcnt vmcnt(%0)\n\ts_barrier" ::"i"(N) : "memory")
#define G1_YW(kt, N) asm volatile("s_waitcnt vmcnt(%2)" : "+v"(y0[kt]), "+v"(y1[kt]) : "i"(N) : "memory");
  G1_BAR(6);
  STAMP(1);
#pragma unroll
  for (int e = 0; e < 6; ++e) fa[e] = G1_RD(0, e);
  G1_LDY(3) G1_STAGE(wsrc + G1_SLOT, 1) G1_YW(0, 11) G1_S(0)
  G1_LDY(4) G1_YW(1, 11) G1_S(1)
  G1_LDY(5) G1_YW(2, 11) G1_S(2)
  G1_LDY(6) G1_YW(3, 11) G1_S(3)
  G1_LDY(7) G1_YW(4, 6) G1_S(4)
  G1_BAR(6); G1_LDY(8) G1_YW(5, 6) G1_S(5)
  G1_LDY(9) G1_STAGE(wsrc + 2 * G1_SLOT, 2) G1_YW(6, 11) G1_S(6)
  G1_LDY(10) G1_YW(7, 11) G1_S(7)
  G1_LDY(11) G1_YW(8, 11) G1_S(8)
  G1_LDY(12) G1_YW(9, 11) G1_S(9)
  G1_LDY(13) G1_YW(10, 6) G1_S(10)
  G1_BAR(6); G1_LDY(14) G1_YW(11, 6) G1_S(11)
  G1_LDY(15) G1_STAGE(wsrc + 3 * G1_SLOT, 0) G1_YW(12, 11) G1_S(12)
  G1_LDY(16) G1_YW(13, 11) G1_S(13)
  G1_LDY(17) G1_YW(14, 11) G1_S(14)
  {
    glds<1>(ws + OFF_UV + n * 4608 + (w < 4 ? w : 4) * 1024, voff, lds0 + G1_UV + (w < 4 ? w : 4) * 1024);
    const char* sp = ws + OFF_ST + (size_t)(rb * 256 + w * 32) * 64;
    const unsigned so = (lane & 15) * 64 + g * 16;
    GLD16(st0, so, sp, 0); GLD16N(st1, so, sp, 1024);
  }
  G1_YW(15, 12) G1_S(15)
  G1_YW(16, 5) G1_S(16)
  G1_BAR(0); G1_YW(17, 0) asm volatile("" : "+v"(st0), "+v"(st1));
  G1_STAGE(wsrc + 4 * G1_SLOT, 1)
  G1_S(17)
  STAMP(2);
  {
    float mu, sd2;
    ln_stats(st0, mu, sd2);
    cr0 = corr_operand(-mu, __builtin_amdgcn_sqrtf(sd2), g);
    ln_stats(st1, mu, sd2);
    cr1 = corr_operand(-mu, __builtin_amdgcn_sqrtf(sd2), g);
    asm volatile("s_nop 1" : "+v"(cr0), "+v"(cr1));
  }
  G1_EPI(0)
  STAMP(3);
#pragma unroll 1
  for (int sw = 1; sw < 3; ++sw) {
    G1_S(0) G1_S(1) G1_S(2) G1_BAR(4); G1_STAGE(wsrc + (3 * sw + 2) * G1_SLOT, 2) G1_S(3) G1_S(4) G1_S(5)
    G1_S(6) G1_S(7) G1_S(8) G1_BAR(0); if (sw == 1) G1_STAGE(wsrc + 6 * G1_SLOT, 0) G1_S(9) G1_S(10) G1_S(11)
    G1_S(12) G1_S(13) G1_S(14) if (sw == 1) { G1_BAR(0); G1_STAGE(wsrc + 7 * G1_SLOT, 1) } G1_S(15) G1_S(16) G1_S(17)
    if (sw == 1) STAMP(4); else STAMP(5);
    G1_EPI(sw)
  }
  STAMP(6); STAMP_FLUSH(4);
}

constexpr int G2_LDS = 4 * 4 * 5 * 1024;
__global__ __launch_bounds__(256, 2) void k_ffn2(char* __restrict__ ws, u16* __restrict__ out) {
  extern __shared__ __attribute__((aligned(16))) char smem[];
  STAMP_DECL; STAMP(0);
  const int lane = threadIdx.x & 63, w = wave_id(), b = blockIdx.x, g = lane >> 4;
  const int xg = b & 7, j = b >> 3;
  const int tb = (xg >> 1) * 8 + (j >> 2), c = (xg & 1) * 4 + (j & 3);
  const char* h0 = ws + OFF_HT + (size_t)(tb * 4) * (576 * 256) + w * 1024;
  const char* h1 = h0 + 576 * 256; const char* h2 = h1 + 576 * 256; const char* h3 = h2 + 576 * 256;
  const char* wsrc = ws + OFF_W2T + (size_t)c * (144 * 4608) + w * 4608;
  unsigned oh = lane * 16, o4 = lane * 16, o5 = 4096 + g * 128 + (lane & 7) * 16;
  f32x4 acc[4][5];
#pragma unroll
  for (int t = 0; t < 4; ++t)
#pragma unroll
    for (int e = 0; e < 5; ++e) acc[t][e] = f32x4{0.f, 0.f, 0.f, 0.f};
  bf16x8 hf[4][4], wf[4][5];
#define G2_LOAD(s)                                                                                          \
  {                                                                                                         \
    GLD16N(wf[s][0], o4, wsrc, 0); GLD16N(hf[s][0], oh, h0, 0); GLD16N(wf[s][1], o4, wsrc, 1024); GLD16N(hf[s][1], oh, h1, 0); \
    GLD16N(wf[s][2], o4, wsrc, 2048); GLD16N(hf[s][2], oh, h2, 0); GLD16N(wf[s][3], o4, wsrc, 3072); GLD16N(hf[s][3], oh, h3, 0); \
    GLD16N(wf[s][4], o5, wsrc, 0);                                                                          \
    oh += 4096; o4 += 4 * 4608; o5 += 4 * 4608;                                                             \
  }
#define G2_MM(s, N)                                                                                         \
  {                                                                                                         \
    asm volatile("s_waitcnt vmcnt(%9)" : "+v"(hf[s][0]), "+v"(hf[s][1]), "+v"(hf[s][2]), "+v"(hf[s][3]), "+v"(wf[s][0]), "+v"(wf[s][1]), \
                 "+v"(wf[s][2]), "+v"(wf[s][3]), "+v"(wf[s][4]) : "i"(N) : "memory");                       \
    _Pragma("unroll") for (int t = 0; t < 4; ++t)                                                           \
      _Pragma("unroll") for (int e = 0; e < 5; ++e) acc[t][e] = MFMA16(wf[s][e], hf[s][t], acc[t][e]);      \
    SBAR();                                                                                                 \
  }
  asm volatile("s_nop 4" : "+s"(h0), "+s"(h1), "+s"(h2), "+s"(h3), "+s"(wsrc));
  G2_LOAD(0) G2_LOAD(1) G2_LOAD(2) G2_LOAD(3)
  SBAR();
#pragma unroll 1
  for (int it = 0; it < 8; ++it) {
    G2_MM(0, 27) G2_LOAD(0) G2_MM(1, 27) G2_LOAD(1) G2_MM(2, 27) G2_LOAD(2) G2_MM(3, 27) G2_LOAD(3)
  }
  G2_MM(0, 27) G2_MM(1, 18) G2_MM(2, 9) G2_MM(3, 0)
  STAMP(1);
  LAS char* rl = (LAS char*)smem + lane * 16;
#pragma unroll
  for (int t = 0; t < 4; ++t)
    if (t != w)
#pragma unroll
      for (int e = 0; e < 5; ++e) *(LAS f32x4*)(rl + ((t * 4 + w) * 5 + e) * 1024) = acc[t][e];
  f32x4 r[5];
#pragma unroll
  for (int t = 0; t < 4; ++t)
    if (t == w)
#pragma unroll
      for (int e = 0; e < 5; ++e) r[e] = acc[t][e];
  const int token = tb * 64 + w * 16 + (lane & 15);
  const char* x1p = ws + OFF_X1 + (size_t)(token >> 4) * (72 * 256) + (token & 15) * 16 + 9 * c * 256;
  const u32x4 xa = *(const u32x4*)(x1p + 2 * g * 256), xb = *(const u32x4*)(x1p + (2 * g + 1) * 256);
  const u32x2 xc = *(const u32x2*)(x1p + 8 * 256 + (g & 1) * 8);
  const float* gbp = (const float*)(ws + OFF_MODF) + 5 * D;
  const int d0 = 72 * c + 16 * g, d1 = 72 * c + 64 + 4 * (g & 1);
  f32x4 gb[5];
#pragma unroll
  for (int i = 0; i < 4; ++i) gb[i] = *(const f32x4*)(gbp + d0 + 4 * i);
  gb[4] = *(const f32x4*)(gbp + d1);
  float rv;
  { float mu, sd2; ln_stats(*(const f32x4*)(ws + OFF_ST + (size_t)token * 64 + g * 16), mu, sd2); rv = __builtin_amdgcn_rcpf(sd2); }
#pragma unroll
  for (int i = 0; i < 5; ++i) gb[i] *= rv;
  __syncthreads();
#pragma unroll
  for (int s = 1; s < 4; ++s) {
    const int ow = (w + s) & 3;
#pragma unroll
    for (int e = 0; e < 5; ++e) r[e] += *(const LAS f32x4*)(rl + ((w * 4 + ow) * 5 + e) * 1024);
  }
  u32x4 oa, ob;
#pragma unroll
  for (int i = 0; i < 4; ++i) {
    const unsigned w0 = i < 2 ? xa[2 * i] : xb[2 * i - 4], w1 = i < 2 ? xa[2 * i + 1] : xb[2 * i - 3];
    const unsigned r0 = pk2(bf_lo(w0) + gb[i][0] * r[0][i], bf_hi(w0) + gb[i][1] * r[1][i]);
    const unsigned r1 = pk2(bf_lo(w1) + gb[i][2] * r[2][i], bf_hi(w1) + gb[i][3] * r[3][i]);
    if (i < 2) { oa[2 * i] = r0; oa[2 * i + 1] = r1; } else { ob[2 * i - 4] = r0; ob[2 * i - 3] = r1; }
  }
  u16* op = out + (size_t)token * D;
  *(u32x4*)(op + d0) = oa; *(u32x4*)(op + d0 + 8) = ob;
  if (g < 2)
    *(u32x2*)(op + d1) = u32x2{pk2(bf_lo(xc[0]) + gb[4][0] * r[4][0], bf_hi(xc[0]) + gb[4][1] * r[4][1]),
                               pk2(bf_lo(xc[1]) + gb[4][2] * r[4][2], bf_hi(xc[1]) + gb[4][3] * r[4][3])};
  STAMP(2); STAMP_FLUSH(5);
}

extern "C" void kernel_launch(void* const* d_in, const int* in_sizes, int n_in, void* d_out, int out_size, void* d_ws, size_t ws_size,
                              hipStream_t stream) {
  const u16* x = (const u16*)d_in[0];
  const u16* wq = (const u16*)d_in[1];
  const u16* wk = (const u16*)d_in[2];
  const u16* wv = (const u16*)d_in[3];
  const u16* wo = (const u16*)d_in[4];
  const u16* wg = (const u16*)d_in[6];
  const u16* wu = (const u16*)d_in[7];
  const u16* wd = (const u16*)d_in[8];
  const u16* cv = (const u16*)d_in[9];
  const u16* wm = (const u16*)d_in[10];
  char* ws = (char*)d_ws;
  (void)hipFuncSetAttribute((const void*)k_qkv, hipFuncAttributeMaxDynamicSharedMemorySize, SG_LDS);
  (void)hipFuncSetAttribute((const void*)k_attn, hipFuncAttributeMaxDynamicSharedMemorySize, ATT_LDS);
  (void)hipFuncSetAttribute((const void*)k_oproj, hipFuncAttributeMaxDynamicSharedMemorySize, SG_LDS);
  (void)hipFuncSetAttribute((const void*)k_ffn1, hipFuncAttributeMaxDynamicSharedMemorySize, G1_LDS);
  (void)hipFuncSetAttribute((const void*)k_ffn2, hipFuncAttributeMaxDynamicSharedMemorySize, G2_LDS);
  k_prep<<<P_GEMV + P_LN + P_WQ + P_WO + P_CONST, 384, 0, stream>>>(x, wq, wk, wv, wo, cv, wm, ws);
  k_qkv<<<256, 512, SG_LDS, stream>>>(ws);
  k_attn<<<256, 768, ATT_LDS, stream>>>(wg, wu, wd, ws);
  k_oproj<<<256, 512, SG_LDS, stream>>>(x, ws);
  k_ffn1<<<256, 512, G1_LDS, stream>>>(ws);
  k_ffn2<<<256, 256, G2_LDS, stream>>>(ws, (u16*)d_out);
}
```

```cpp
#include <hip/hip_runtime.h>
#include <hip/hip_bf16.h>
#include <stdint.h>

typedef unsigned short u16;
typedef short bf16x8 __attribute__((ext_vector_type(8)));
typedef short bf16x4 __attribute__((ext_vector_type(4)));
typedef float f32x2 __attribute__((ext_vector_type(2)));
typedef float f32x4 __attribute__((ext_vector_type(4)));
typedef float f32x16 __attribute__((ext_vector_type(16)));
typedef unsigned u32x2 __attribute__((ext_vector_type(2)));
typedef unsigned u32x4 __attribute__((ext_vector_type(4)));
typedef __bf16 bf2_t __attribute__((ext_vector_type(2)));
#define LAS __attribute__((address_space(3)))
#define DI __device__ __forceinline__
#define SBAR() __builtin_amdgcn_sched_barrier(0)
#define PIN(x) asm volatile("" : "+v"(x))
#define MFMA16(a, b, c) __builtin_amdgcn_mfma_f32_16x16x32_bf16(a, b, c, 0, 0, 0)
#define MFMA32(a, b, c) __builtin_amdgcn_mfma_f32_32x32x16_bf16(a, b, c, 0, 0, 0)
#define MFMA32K8(a, b, c) __builtin_amdgcn_mfma_f32_32x32x8bf16_1k(a, b, c, 0, 0, 0)

constexpr int S = 2048, D = 576, NH = 32, DH = 18, F = 4608;
constexpr float LN_EPS = 1e-5f;

constexpr size_t ACT_B = (size_t)S * D * 2;
constexpr int KV_TILE = 2496;
constexpr size_t OFF_MODF = 0;
constexpr size_t OFF_MODB = 16384;
constexpr size_t OFF_MODP = 32768;
constexpr size_t OFF_N1 = 98304;
constexpr size_t OFF_WQT = OFF_N1 + ACT_B;
constexpr size_t WT_B = 8 * 18 * 5 * 1024;
constexpr size_t OFF_WOT = OFF_WQT + WT_B;
constexpr size_t OFF_Q1 = OFF_WOT + WT_B;
constexpr size_t OFF_Q2 = OFF_Q1 + (size_t)NH * S * 32;
constexpr size_t OFF_KV = OFF_Q2 + (size_t)NH * S * 4;
constexpr size_t OFF_OT = OFF_KV + 160 * 1024;
constexpr size_t OFF_X1 = OFF_OT + ACT_B;
constexpr size_t OFF_Y = OFF_X1 + ACT_B;
constexpr size_t OFF_ST = OFF_Y + ACT_B;
constexpr size_t OFF_UVP = OFF_ST + (size_t)S * 64;
constexpr size_t OFF_UV = OFF_UVP + (size_t)18 * 9216 * 8;
constexpr size_t OFF_W1T = OFF_UV + (size_t)9216 * 16;
constexpr size_t OFF_W2T = OFF_W1T + (size_t)144 * 18 * 4096;
constexpr size_t OFF_HT = OFF_W2T + (size_t)8 * 144 * 4608;
constexpr size_t OFF_XB = OFF_HT + (size_t)S * F * 2;
constexpr size_t OFF_CNT = OFF_XB + (size_t)64 * 16 * 9216;
constexpr size_t WS_END = OFF_CNT + 768 * 128;

DI float bf_lo(unsigned w) { return __uint_as_float(w << 16); }
DI float bf_hi(unsigned w) { return __uint_as_float(w & 0xffff0000u); }
DI float bf2f(u16 b) { return __uint_as_float((unsigned)b << 16); }
DI unsigned pk2(float lo, float hi) { f32x2 v = {lo, hi}; return __builtin_bit_cast(unsigned, __builtin_convertvector(v, bf2_t)); }
typedef _Float16 h2_t __attribute__((ext_vector_type(2)));
DI unsigned pkh(f32x2 v) { return __builtin_bit_cast(unsigned, __builtin_convertvector(v, h2_t)); }
template <int HI> DI float fma_h(unsigned w, float c, float s) {
  float r;
  if (HI) asm("v_fma_mix_f32 %0, %1, %2, %3 op_sel:[1,0,0] op_sel_hi:[1,0,0]" : "=v"(r) : "v"(w), "v"(c), "v"(s));
  else asm("v_fma_mix_f32 %0, %1, %2, %3 op_sel_hi:[1,0,0]" : "=v"(r) : "v"(w), "v"(c), "v"(s));
  return r;
}
DI float rbf(float x) { return bf_lo(pk2(x, x)); }
DI unsigned plo(unsigned a, unsigned b) { return __builtin_amdgcn_perm(b, a, 0x05040100u); }
DI unsigned phi(unsigned a, unsigned b) { return __builtin_amdgcn_perm(b, a, 0x07060302u); }
DI float RELU(float x) { float r; asm("v_max_f32 %0, 0, %1" : "=v"(r) : "v"(x)); return r; }
DI int wave_id() { return __builtin_amdgcn_readfirstlane(threadIdx.x >> 6); }

template <int N> DI void glds(const void* sbase, unsigned voff, unsigned lds) {
  unsigned keep;
  if constexpr (N == 1)
    asm volatile("s_mov_b32 %0, m0\n\ts_mov_b32 m0, %3\n\ts_nop 2\n\tglobal_load_lds_dwordx4 %1, %2\n\ts_mov_b32 m0, %0"
                 : "=&s"(keep) : "v"(voff), "s"(sbase), "s"(lds) : "memory");
  if constexpr (N == 2)
    asm volatile("s_mov_b32 %0, m0\n\ts_mov_b32 m0, %3\n\ts_nop 2\n\tglobal_load_lds_dwordx4 %1, %2\n\tglobal_load_lds_dwordx4 %1, %2 offset:1024\n\ts_mov_b32 m0, %0"
                 : "=&s"(keep) : "v"(voff), "s"(sbase), "s"(lds) : "memory");
  if constexpr (N == 4)
    asm volatile("s_mov_b32 %0, m0\n\ts_mov_b32 m0, %3\n\ts_nop 2\n\tglobal_load_lds_dwordx4 %1, %2\n\tglobal_load_lds_dwordx4 %1, %2 offset:1024\n\t"
                 "global_load_lds_dwordx4 %1, %2 offset:2048\n\tglobal_load_lds_dwordx4 %1, %2 offset:3072\n\ts_mov_b32 m0, %0"
                 : "=&s"(keep) : "v"(voff), "s"(sbase), "s"(lds) : "memory");
}
DI void glds45(const void* sbase, unsigned voff, unsigned lds) {
  unsigned keep;
  asm volatile("s_mov_b32 %0, m0\n\ts_mov_b32 m0, %3\n\ts_mov_b32 exec_hi, 0\n\ts_nop 1\n\tglobal_load_lds_dwordx4 %1, %2\n\ts_mov_b32 exec_hi, -1\n\t"
               "global_load_lds_dwordx4 %1, %2 offset:512\n\tglobal_load_lds_dwordx4 %1, %2 offset:1536\n\t"
               "global_load_lds_dwordx4 %1, %2 offset:2560\n\tglobal_load_lds_dwordx4 %1, %2 offset:3584\n\ts_mov_b32 m0, %0"
               : "=&s"(keep) : "v"(voff), "s"(sbase), "s"(lds) : "memory");
}
template <int J> DI void glds45p(const void* sbase, unsigned voff, unsigned lds) {
  unsigned keep;
  if constexpr (J == 0)
    asm volatile("s_mov_b32 %0, m0\n\ts_mov_b32 m0, %3\n\ts_mov_b32 exec_hi, 0\n\ts_nop 1\n\tglobal_load_lds_dwordx4 %1, %2\n\ts_mov_b32 exec_hi, -1\n\ts_mov_b32 m0, %0"
                 : "=&s"(keep) : "v"(voff), "s"(sbase), "s"(lds) : "memory");
  else
    asm volatile("s_mov_b32 %0, m0\n\ts_mov_b32 m0, %3\n\ts_nop 2\n\tglobal_load_lds_dwordx4 %1, %2 offset:%4\n\ts_mov_b32 m0, %0"
                 : "=&s"(keep) : "v"(voff), "s"(sbase), "s"(lds), "i"(J * 1024 - 512) : "memory");
}
DI void st16_wt(void* p, u32x4 v) { asm volatile("global_store_dwordx4 %0, %1, off sc1\n\ts_nop 1" ::"v"(p), "v"(v) : "memory"); }
DI void st8_wt(void* p, u32x2 v) { asm volatile("global_store_dwordx2 %0, %1, off sc1" ::"v"(p), "v"(v) : "memory"); }
DI void st16_wts(const char* sb, unsigned vo, u32x4 v) { asm volatile("s_nop 4\n\tglobal_store_dwordx4 %0, %1, %2 sc1\n\ts_nop 1" ::"v"(vo), "v"(v), "s"(sb) : "memory"); }
DI void st8_wts(const char* sb, unsigned vo, u32x2 v) { asm volatile("s_nop 4\n\tglobal_store_dwordx2 %0, %1, %2 sc1" ::"v"(vo), "v"(v), "s"(sb) : "memory"); }
#define AMFMA16(acc, a, b) asm volatile("v_mfma_f32_16x16x32_bf16 %0, %1, %2, %0" : "+v"(acc) : "v"(a), "v"(b))
#define AMFMA16Z(acc, a, b) asm volatile("v_mfma_f32_16x16x32_bf16 %0, %1, %2, 0" : "=&v"(acc) : "v"(a), "v"(b))
#define GLD16(dst, voff, sbase, imm) asm volatile("s_nop 4\n\tglobal_load_dwordx4 %0, %1, %2 offset:%3" : "=v"(dst) : "v"(voff), "s"(sbase), "i"(imm) : "memory")
#define GLD16N(dst, voff, sbase, imm) asm volatile("global_load_dwordx4 %0, %1, %2 offset:%3" : "=v"(dst) : "v"(voff), "s"(sbase), "i"(imm) : "memory")
#define LDS_BAR() asm volatile("s_waitcnt lgkmcnt(0)\n\ts_barrier" ::: "memory")

DI void wait_count(const char* p, unsigned need) {
  unsigned spins = 0;
  while ((unsigned)__builtin_amdgcn_readfirstlane(__hip_atomic_load((const unsigned*)p, __ATOMIC_RELAXED, __HIP_MEMORY_SCOPE_AGENT)) < need &&
         ++spins < (1u << 22))
    __builtin_amdgcn_s_sleep(1);
}
DI void wait_count_fast(const char* p, unsigned need) {
  unsigned v0, v1, v2, v3;
  const unsigned z = 0;
#define POLL_LD(v) asm volatile("global_load_dword %0, %1, %2 sc1\n\ts_sleep 1" : "=v"(v) : "v"(z), "s"(p) : "memory")
#define POLL_CK(v) asm volatile("s_waitcnt vmcnt(3)" : "+v"(v)::"memory"); if ((unsigned)__builtin_amdgcn_readfirstlane(v) >= need) break; POLL_LD(v);
  asm volatile("s_nop 4" : "+s"(p));
  POLL_LD(v0); POLL_LD(v1); POLL_LD(v2); POLL_LD(v3);
#pragma unroll 1
  for (unsigned spins = 0; spins < (1u << 21); ++spins) { POLL_CK(v0) POLL_CK(v1) POLL_CK(v2) POLL_CK(v3) }
  asm volatile("s_barrier\n\ts_waitcnt vmcnt(0)" : "+v"(v0), "+v"(v1), "+v"(v2), "+v"(v3)::"memory");
}
DI void add_count(char* p) { __hip_atomic_fetch_add((unsigned*)p, 1u, __ATOMIC_RELAXED, __HIP_MEMORY_SCOPE_AGENT); }
#define GLD16S(dst, voff, sbase, imm) asm volatile("global_load_dwordx4 %0, %1, %2 offset:%3 sc1" : "=v"(dst) : "v"(voff), "s"(sbase), "i"(imm) : "memory")

#define CODE_ADDR(var, sym) const char* var; { int d_; asm volatile("s_getpc_b64 %0\n\ts_mov_b32 %1, " sym "@rel32@lo+4" : "=s"(var), "=s"(d_)); var += d_; }
DI void touch_code(const char* from, const char* to, unsigned idx) {
  const unsigned len = to > from ? (unsigned)(to - from) : 0u;
  if (idx * 128u < len) { unsigned t; asm volatile("global_load_dword %0, %1, off\n\ts_waitcnt vmcnt(0)" : "=&v"(t) : "v"(from + idx * 128u) : "memory"); }
}

DI void quad_transpose(const u32x2 (&L)[8], u32x4 (&T)[4]) {
#pragma unroll
  for (int m = 0; m < 4; ++m) {
    T[0][m] = plo(L[2 * m][0], L[2 * m + 1][0]);
    T[1][m] = phi(L[2 * m][0], L[2 * m + 1][0]);
    T[2][m] = plo(L[2 * m][1], L[2 * m + 1][1]);
    T[3][m] = phi(L[2 * m][1], L[2 * m + 1][1]);
  }
}
template <int N> DI float ror_add(float x) {
  return x + __uint_as_float(__builtin_amdgcn_update_dpp(0u, __float_as_uint(x), 0x120 + N, 0xf, 0xf, false));
}
DI float row_sum(float x) { return ror_add<1>(ror_add<2>(ror_add<4>(ror_add<8>(x)))); }
DI float swap32_add(float a, float b) {
  const auto r = __builtin_amdgcn_permlane32_swap(__float_as_uint(a), __float_as_uint(b), false, false);
  return __uint_as_float(r[0]) + __uint_as_float(r[1]);
}
DI float swap16_add(float a, float b) {
  const auto r = __builtin_amdgcn_permlane16_swap(__float_as_uint(a), __float_as_uint(b), false, false);
  return __uint_as_float(r[0]) + __uint_as_float(r[1]);
}

extern "C" __global__ __launch_bounds__(640) void k_prep(const u16* __restrict__ x, const u16* __restrict__ wq, const u16* __restrict__ wk,
                                             const u16* __restrict__ wv, const u16* __restrict__ wo, const u16* __restrict__ cvec,
                                             const u16* __restrict__ wmod, char* __restrict__ ws) {
  __shared__ float red[6][64];
  __shared__ unsigned arrived;
  const int tid = threadIdx.x, lane = tid & 63, w = wave_id();
  const unsigned b = blockIdx.x;
  if (tid == 0) arrived = 0u;
  __syncthreads();
  if (w >= 8 || (w >= 4 && w < 6 && b >= 224)) {
    const unsigned r = lane & 15, g = lane >> 4;
    const unsigned un = w >= 8 ? b : b + 32;
    const bool isq = un < 144;
    const unsigned unit = isq ? un : un - 144, c = unit & 7, kt = unit >> 3;
    const char* W = (const char*)(isq ? wq : wo);
    const unsigned P = 4 * kt + g, pc = (P * 57) >> 9, pi = P - 9 * pc;
    const unsigned k0 = isq ? 8 * P : 72 * pc + (pi < 8 ? 18 * (pi >> 1) + 8 * (pi & 1) : 16), A = (!isq && pi == 8) ? 18 : 2;
    char* dst = ws + (isq ? OFF_WQT : OFF_WOT) + (c * 18 + kt) * 5120 + lane * 16;
    if (!(w & 1)) {
      const unsigned col = isq ? 72 * c + 18 * (r >> 2) + 4 * (r & 3) : 72 * c + 4 * r;
      const unsigned o0 = (k0 * D + col) * 2, os = A * (D * 2);
      u32x2 L[8];
#pragma unroll
      for (int m = 0; m < 4; ++m) {
        const unsigned* p = (const unsigned*)(W + (o0 + m * os));
        L[2 * m][0] = p[0]; L[2 * m][1] = p[1]; L[2 * m + 1][0] = p[D / 2]; L[2 * m + 1][1] = p[D / 2 + 1];
      }
      u32x4 T[4];
      quad_transpose(L, T);
#pragma unroll
      for (int e = 0; e < 4; ++e) *(u32x4*)(dst + e * 1024) = T[e];
    } else {
      const unsigned rr = r & 7;
      const bool hi = r >= 8, usev = c > 2 || (c == 2 && rr >= 4);
      const bool ev = !hi || (isq && (c == 2 ? (rr & 3) < 2 : c < 5));
      const unsigned ecol = hi ? (c == 2 ? 16 + (rr & 1) : 8 * ((c >= 3 ? c - 3 : c) & 1) + rr) : (isq ? 72 * c + 18 * (rr >> 1) + 16 + (rr & 1) : 72 * c + 64 + rr);
      const unsigned ep = hi ? DH * 2 : D * 2;
      const char* pe = (hi ? (const char*)(usev ? wv : wk) : W) + (k0 * ep + ecol * 2);
      unsigned E[8];
#pragma unroll
      for (int m = 0; m < 4; ++m) {
        const char* q = pe + m * A * ep;
        E[2 * m] = *(const u16*)q; E[2 * m + 1] = *(const u16*)(q + ep);
      }
      u32x4 t4 = {plo(E[0], E[1]), plo(E[2], E[3]), plo(E[4], E[5]), plo(E[6], E[7])};
      if (!ev) t4 = u32x4{0u, 0u, 0u, 0u};
      *(u32x4*)(dst + 4096) = t4;
    }
    return;
  }
  if (w >= 6) {
    const unsigned token = (b & 7) * 256 + (b >> 3) * 8 + (w - 6) * 4 + (lane >> 4), sub = lane & 15;
    u32x2 xv[9];
#pragma unroll
    for (int i = 0; i < 9; ++i) xv[i] = *(const u32x2*)(x + token * D + (sub + 16 * i) * 4);
    f32x2 v[18], s = {0.f, 0.f};
#pragma unroll
    for (int i = 0; i < 9; ++i) { v[2 * i] = f32x2{bf_lo(xv[i][0]), bf_hi(xv[i][0])}; v[2 * i + 1] = f32x2{bf_lo(xv[i][1]), bf_hi(xv[i][1])}; }
#pragma unroll
    for (int i = 0; i < 18; ++i) s += v[i];
    const float mean = row_sum(s[0] + s[1]) * (1.f / D);
    f32x2 q = {0.f, 0.f};
#pragma unroll
    for (int i = 0; i < 18; ++i) { v[i] -= mean; q += v[i] * v[i]; }
    const float rstd = __builtin_amdgcn_rsqf(row_sum(q[0] + q[1]) * (1.f / D) + LN_EPS);
    char* dst = ws + OFF_N1 + (token >> 4) * (72 * 256) + (token & 15) * 16 + (sub >> 1) * 256 + (sub & 1) * 8;
#pragma unroll
    for (int i = 0; i < 9; ++i) {
      const f32x2 a = v[2 * i] * rstd, c2 = v[2 * i + 1] * rstd;
      *(u32x2*)(dst + i * 2048) = u32x2{pkh(a), pkh(c2)};
    }
    return;
  }
  if (b < 216) {
    const unsigned strip = b >> 2, kq = b & 3, r0 = kq * 144 + (tid >> 3);
    u32x4 wv4[3]; float sc[3];
#pragma unroll
    for (int p = 0; p < 3; ++p) wv4[p] = __builtin_nontemporal_load((const u32x4*)(wmod + (r0 + 48 * p) * 3456 + strip * 64 + (tid & 7) * 8));
#pragma unroll
    for (int p = 0; p < 3; ++p) { const float cv = bf2f(cvec[r0 + 48 * p]); sc[p] = rbf(cv * __builtin_amdgcn_rcpf(1.f + __expf(-cv))); }
    float acc[8];
#pragma unroll
    for (int j = 0; j < 4; ++j) {
      acc[2 * j] = sc[0] * bf_lo(wv4[0][j]) + sc[1] * bf_lo(wv4[1][j]) + sc[2] * bf_lo(wv4[2][j]);
      acc[2 * j + 1] = sc[0] * bf_hi(wv4[0][j]) + sc[1] * bf_hi(wv4[1][j]) + sc[2] * bf_hi(wv4[2][j]);
    }
    const float a0 = swap32_add(acc[0], acc[4]), a1 = swap32_add(acc[1], acc[5]), a2 = swap32_add(acc[2], acc[6]), a3 = swap32_add(acc[3], acc[7]);
    const float b0 = ror_add<8>(swap16_add(a0, a2)), b1 = ror_add<8>(swap16_add(a1, a3));
    *(f32x2*)&red[w][(lane & 7) * 8 + (lane >> 4) * 2] = f32x2{b0, b1};
    unsigned old = 0;
    if (lane == 0) old = __hip_atomic_fetch_add(&arrived, 1u, __ATOMIC_ACQ_REL, __HIP_MEMORY_SCOPE_WORKGROUP);
    if (__builtin_amdgcn_readfirstlane(old) == 5)
      ((float*)(ws + OFF_MODP))[kq * 3456 + strip * 64 + lane] = ((red[0][lane] + red[1][lane]) + (red[2][lane] + red[3][lane])) + (red[4][lane] + red[5][lane]);
    return;
  }
  if (b < 224) {
    CODE_ADDR(c0, "k_qkv"); CODE_ADDR(c1, "k_end");
    touch_code(c0, c1, tid);
  } else if (b < 232) {
    const unsigned i = (b - 224) * 64 + lane;
    if (w == 1) *(unsigned*)(ws + OFF_CNT + i * 128) = 0u;
    if (w == 0 && i < 256) *(unsigned*)(ws + OFF_CNT + (512 + i) * 128) = 0u;
    if (w == 3) {
      CODE_ADDR(d0, "k_prep.kd"); CODE_ADDR(d1, "k_end.kd");
      const char* ka = (const char*)((uintptr_t)__builtin_amdgcn_kernarg_segment_ptr() & ~(uintptr_t)4095);
      if (lane < 32) touch_code(ka, ka + 4096, lane); else touch_code(d0, d1 + 64, lane - 32);
    }
    if (w == 2 && i < 256) *(u32x4*)(ws + OFF_KV + (i >> 2) * KV_TILE + 1024 + (i & 3) * 304 + 18 * 16) = u32x4{0x3f803f80u, 0x3f803f80u, 0x3f803f80u, 0x3f803f80u};
  }
}
DI float mod_final(float s, bool plus1) { s = rbf(s); return plus1 ? rbf(1.f + s) : s; }

constexpr int SG_LDS = 92160 + 4608;
extern "C" __global__ __launch_bounds__(512) void k_qkv(char* __restrict__ ws) {
  extern __shared__ __attribute__((aligned(16))) char smem[];
  const int b = blockIdx.x, c = b >> 5, tb = (b & 7) * 4 + ((b >> 3) & 3);
  f32x4 acc[5];
  {
    const int lane = threadIdx.x & 63, w = wave_id(), tt = w & 3, kh = w >> 2, g = lane >> 4;
    const unsigned voff = lane * 16, lds0 = (unsigned)(uintptr_t)smem;
    const unsigned mx = threadIdx.x + (threadIdx.x < 288 ? 0 : b * 224), mo = (mx < 864 ? mx : 863) * 16;
    const bool domod = w < 5 || b < 3;
    f32x4 mp[4], mv = {};
    const char* modp = ws + OFF_MODP;
    if (domod) { GLD16(mp[0], mo, modp, 0); const unsigned m1 = mo + 13824, m2 = mo + 27648, m3 = mo + 41472;
                 GLD16N(mp[1], m1, modp, 0); GLD16N(mp[2], m2, modp, 0); GLD16N(mp[3], m3, modp, 0); }
    const char* asrc = ws + OFF_N1 + (size_t)(tb * 4 + tt) * (72 * 256) + kh * 9216;
    u32x4 a[9];
    const unsigned v1 = voff + 4096, v2 = voff + 8192;
    GLD16(a[0], voff, asrc, 0); GLD16N(a[1], voff, asrc, 1024); GLD16N(a[2], voff, asrc, 2048);
    const int pofs = kh * 46080 + (tt == 3 ? 11 : 4 * tt) * 1024;
    const char* wsrc = ws + OFF_WQT + (size_t)c * 92160 + pofs;
    glds<4>(wsrc, voff, lds0 + pofs);
    if (domod) {
      asm volatile("s_waitcnt vmcnt(7)" : "+v"(mp[0]), "+v"(mp[1]), "+v"(mp[2]), "+v"(mp[3])::"memory");
      const f32x4 ms = (mp[0] + mp[1]) + (mp[2] + mp[3]);
      const bool p1 = (mx >= 144 && mx < 288) || (mx >= 576 && mx < 720);
      mv = f32x4{mod_final(ms[0], p1), mod_final(ms[1], p1), mod_final(ms[2], p1), mod_final(ms[3], p1)};
      if (mx < 288) *(LAS f32x4*)((LAS char*)smem + 92160 + mo) = mv;
    }
    LDS_BAR();
    const LAS char* wl = (const LAS char*)smem + kh * 46080 + voff;
    const LAS char* ml = (const LAS char*)smem + 92160 + (kh * 288 + g * 8) * 4;
    f32x4 mf[3][4];
    bf16x8 wf[2][5];
#define QK_MRD(i) { mf[(i) % 3][0] = *(const LAS f32x4*)(ml + (i) * 128); mf[(i) % 3][2] = *(const LAS f32x4*)(ml + 2304 + (i) * 128); \
      mf[(i) % 3][1] = *(const LAS f32x4*)(ml + (i) * 128 + 16); mf[(i) % 3][3] = *(const LAS f32x4*)(ml + 2304 + (i) * 128 + 16); SBAR(); }
#define QK_MOD(i, N)                                                                                 \
    {                                                                                                \
      if ((i) + 2 < 9) QK_MRD((i) + 2)                                                               \
      asm volatile("s_waitcnt vmcnt(%1)" : "+v"(a[i]) : "i"(N) : "memory");                          \
      const f32x4 s0 = mf[(i) % 3][0], s1 = mf[(i) % 3][1], c0 = mf[(i) % 3][2], c1 = mf[(i) % 3][3]; \
      const u32x4 n = a[i];                                                                          \
      const float t0 = fma_h<0>(n[0], c0[0], s0[0]), t1 = fma_h<1>(n[0], c0[1], s0[1]), t2 = fma_h<0>(n[1], c0[2], s0[2]), t3 = fma_h<1>(n[1], c0[3], s0[3]); \
      const float t4 = fma_h<0>(n[2], c1[0], s1[0]), t5 = fma_h<1>(n[2], c1[1], s1[1]), t6 = fma_h<0>(n[3], c1[2], s1[2]), t7 = fma_h<1>(n[3], c1[3], s1[3]); \
      a[i] = u32x4{pk2(t0, t1), pk2(t2, t3), pk2(t4, t5), pk2(t6, t7)};                              \
      PIN(a[i]); SBAR();                                                                             \
    }
#define QK_WRD(i) { _Pragma("unroll") for (int e = 0; e < 5; ++e) wf[(i) & 1][e] = *(const LAS bf16x8*)(wl + (i) * 5120 + e * 1024); SBAR(); }
#define QK_MM(i) { _Pragma("unroll") for (int e = 0; e < 5; ++e) acc[e] = MFMA16(wf[(i) & 1][e], __builtin_bit_cast(bf16x8, a[i]), acc[e]); SBAR(); }
#pragma unroll
    for (int e = 0; e < 5; ++e) acc[e] = f32x4{0.f, 0.f, 0.f, 0.f};
    QK_MRD(0) QK_MRD(1)
    GLD16N(a[3], voff, asrc, 3072); GLD16N(a[4], v1, asrc, 0); GLD16N(a[5], v1, asrc, 1024);
    glds<4>(wsrc + 15360, voff, lds0 + pofs + 15360);
    QK_MOD(0, 13) QK_MOD(1, 12)
    GLD16N(a[6], v1, asrc, 2048); GLD16N(a[7], v1, asrc, 3072); GLD16N(a[8], v2, asrc, 0);
    QK_MOD(2, 14)
    asm volatile("s_waitcnt vmcnt(10)\n\ts_barrier" ::: "memory");
    QK_WRD(0) QK_WRD(1)
    glds<4>(wsrc + 30720, voff, lds0 + pofs + 30720);
    QK_MM(0) QK_WRD(2) QK_MM(1) QK_MOD(3, 13) QK_MOD(4, 12) QK_MOD(5, 11)
    asm volatile("s_waitcnt vmcnt(7)\n\ts_barrier" ::: "memory");
    QK_WRD(3) QK_MM(2) QK_WRD(4) QK_MOD(6, 6) QK_MM(3) QK_WRD(5) QK_MOD(7, 5) QK_MM(4) QK_MOD(8, 4)
    asm volatile("s_waitcnt vmcnt(0)\n\ts_barrier" ::: "memory");
    QK_WRD(6) QK_MM(5) QK_WRD(7) QK_MM(6) QK_WRD(8) QK_MM(7) QK_MM(8)
    LAS char* rl = (LAS char*)smem + (tt & 1) * 5120 + (tt >> 1) * 46080 + voff;
    if (kh == 1)
#pragma unroll
      for (int e = 0; e < 5; ++e) *(LAS f32x4*)(rl + e * 1024) = acc[e];
    LDS_BAR();
    if (kh == 0)
#pragma unroll
      for (int e = 0; e < 5; ++e) acc[e] += *(const LAS f32x4*)(rl + e * 1024);
    else if (mx >= 288 && mx < 864) {
      *(f32x4*)(ws + OFF_MODF + mo) = mv;
      *(u32x2*)(ws + OFF_MODB + mo / 2) = u32x2{pk2(mv[0], mv[1]), pk2(mv[2], mv[3])};
    }
  }
  const int lane = threadIdx.x & 63, w = threadIdx.x >> 6;
  if (w >= 4) return;
  const int g = lane >> 4, token = tb * 64 + w * 16 + (lane & 15);
  constexpr float QS = 0.23570226039551584f * 1.4426950408889634f;
  u32x4 q0, q1;
  q0[0] = pk2(acc[0][0] * QS, acc[1][0] * QS); q0[1] = pk2(acc[2][0] * QS, acc[3][0] * QS);
  q0[2] = pk2(acc[0][1] * QS, acc[1][1] * QS); q0[3] = pk2(acc[2][1] * QS, acc[3][1] * QS);
  q1[0] = pk2(acc[0][2] * QS, acc[1][2] * QS); q1[1] = pk2(acc[2][2] * QS, acc[3][2] * QS);
  q1[2] = pk2(acc[0][3] * QS, acc[1][3] * QS); q1[3] = pk2(acc[2][3] * QS, acc[3][3] * QS);
  char* qd = ws + OFF_Q1 + ((size_t)(4 * c + g) * S + token) * 32;
  *(u32x4*)qd = q0; *(u32x4*)(qd + 16) = q1;
  const f32x4 t = acc[4];
  if (g < 2) {
    unsigned* q2 = (unsigned*)(ws + OFF_Q2);
    q2[(size_t)(4 * c + 2 * g) * S + token] = pk2(t[0] * QS, t[1] * QS);
    q2[(size_t)(4 * c + 2 * g + 1) * S + token] = pk2(t[2] * QS, t[3] * QS);
  } else {
    char* kv = ws + OFF_KV + (size_t)(token >> 5) * KV_TILE;
    const int key = token & 31, k16 = token & 15;
    char* vrow = kv + 1024 + (key >> 4) * 608 + ((k16 >> 2) & 1) * 304 + ((k16 & 3) + 4 * (k16 >> 3)) * 2;
    const unsigned p0 = pk2(t[0], t[1]), p1 = pk2(t[2], t[3]);
    if (c < 2) {
      *(u32x2*)(kv + (key + 32 * c) * 16 + (g - 2) * 8) = u32x2{p0, p1};
    } else if (c == 2) {
      if (g == 2) *(u32x2*)(kv + 2240 + key * 8) = u32x2{p0, 0x00003f80u};
      else { *(u16*)(vrow + 16 * 16) = (u16)p0; *(u16*)(vrow + 17 * 16) = (u16)(p0 >> 16); }
    } else if (c < 5) {
      char* vr = vrow + (4 * (c - 3) + 8 * (g - 2)) * 16;
      *(u16*)(vr) = (u16)p0; *(u16*)(vr + 16) = (u16)(p0 >> 16); *(u16*)(vr + 32) = (u16)p1; *(u16*)(vr + 48) = (u16)(p1 >> 16);
    }
  }
}

constexpr int OPF = 5, OPW = 4;
constexpr int ATT_PROG = 64 * KV_TILE, ATT_ZERO = ATT_PROG + 16, ATT_LDS = ATT_ZERO + 624, OP_MOD = 92160, OP_SCR = OP_MOD + 8192;
DI void w1_place(unsigned Q, unsigned half, unsigned& slot, unsigned& frag0, unsigned& T) {
  const unsigned ksl = Q / 36, p = Q - 36 * ksl;
  const unsigned n = p < 16 ? p >> 1 : (p < 24 ? p - 16 : (p < 28 ? 2 * (p - 24) + half : p - 28));
  const unsigned base = p < 16 ? 0 : (p < 28 ? 8 : 14);
  const unsigned t = p < 16 ? 4 * (p & 1) + 2 * half : ((p >= 24 && p < 28) ? 4 : 2 * half);
  T = p < 16 ? 8 : (p < 28 ? 6 : 4);
  slot = (8 * ksl + n) * 18 + base + t;
  frag0 = (8 * ksl + n) * 324 + base * 18 + t;
}
#define LD8NT(dst, vo, sb) asm volatile("global_load_dwordx2 %0, %1, %2 nt" : "=v"(dst) : "v"(vo), "s"(sb) : "memory")
#define ST16W(vo, v, sb, imm) asm volatile("global_store_dwordx4 %0, %1, %2 offset:%3 sc1" ::"v"(vo), "v"(v), "s"(sb), "i"(imm) : "memory")
DI void side_unit(unsigned did, unsigned lane, const u16* wg, const u16* wu, const u16* wd, char* ws) {
  const unsigned r16 = lane & 15, g = lane >> 4, voff = lane * 16;
  const bool isw1 = did < 1296, mainp = did < 1872;
  const unsigned kt = isw1 ? did / 72 : (mainp ? (did - 1296) >> 2 : (did - 1872) * 2), sp = did - 72 * kt;
  const bool shared = !isw1 && (kt % 36) >> 2 == 6;
  const char *pa, *pb;
  unsigned vo, r1, r4;
  if (isw1) {
    const unsigned o = (32 * kt * F + 64 * sp) * 2;
    pa = (const char*)wg + o; pb = (const char*)wu + o;
    vo = g * (8 * F * 2) + r16 * 8; r1 = F * 2; r4 = 4 * F * 2;
  } else {
    pa = (const char*)wd + ((64 * (kt >> 1) + 2 * (kt & 1)) * D + (mainp ? 144 * (did & 3) : 128)) * 2;
    pb = pa + (mainp ? 128 : 2 * D * 2);
    vo = (shared ? 32 * (g & 1) + (g >> 1) : 16 * g) * (D * 2) + (mainp ? r16 * 8 : (r16 >> 2) * 288 + (r16 & 3) * 8);
    r1 = 4 * D * 2; r4 = (shared ? 16 : 1) * D * 2;
  }
  u32x2 La[8], Lb[8];
  u32x4 mv;
  asm volatile("s_nop 4" : "+s"(pa), "+s"(pb));
#pragma unroll
  for (int j = 0; j < 8; ++j) {
    const unsigned ro = (j & 3) * r1 + (j >> 2) * r4;
    const char* qa = pa + ro; const char* qb = pb + ro;
    LD8NT(La[j], vo, qa); LD8NT(Lb[j], vo, qb);
  }
  { const char* mb = ws + OFF_MODB + (isw1 ? 64 * kt : 0); const unsigned mo = ((r16 & 1) ? 3 : 4) * (D * 2) + g * 16; GLD16N(mv, mo, mb, 0); }
  unsigned f0, f1, f2, f3;
  if (isw1) {
    unsigned sl, T;
    w1_place(2 * sp, 0, sl, f0, T); f0 += kt * T; w1_place(2 * sp, 1, sl, f1, T); f1 += kt * T;
    w1_place(2 * sp + 1, 0, sl, f2, T); f2 += kt * T; w1_place(2 * sp + 1, 1, sl, f3, T); f3 += kt * T;
  } else f0 = f1 = f2 = f3 = ((mainp ? (did & 3) * 144 : 0) + kt) * 9;
  asm volatile("s_waitcnt vmcnt(0)" : "+v"(La[0]), "+v"(La[1]), "+v"(La[2]), "+v"(La[3]), "+v"(La[4]), "+v"(La[5]), "+v"(La[6]), "+v"(La[7]),
               "+v"(Lb[0]), "+v"(Lb[1]), "+v"(Lb[2]), "+v"(Lb[3]), "+v"(Lb[4]), "+v"(Lb[5]), "+v"(Lb[6]), "+v"(Lb[7]), "+v"(mv)::"memory");
  u32x4 Ta[4], Tb[4];
  quad_transpose(La, Ta); quad_transpose(Lb, Tb);
  if (isw1) {
    const char* d = ws + OFF_W1T;
    const char *d0 = d + f0 * 1024, *d1 = d + f1 * 1024, *d2 = d + f2 * 1024, *d3 = d + f3 * 1024;
    ST16W(voff, Ta[0], d0, 0); ST16W(voff, Tb[0], d0, 1024); ST16W(voff, Ta[1], d1, 0); ST16W(voff, Tb[1], d1, 1024);
    ST16W(voff, Ta[2], d2, 0); ST16W(voff, Tb[2], d2, 1024); ST16W(voff, Ta[3], d3, 0); ST16W(voff, Tb[3], d3, 1024);
    f32x4 da[4], db[4];
#pragma unroll
    for (int e = 0; e < 4; ++e) {
      da[e] = MFMA16(__builtin_bit_cast(bf16x8, mv), __builtin_bit_cast(bf16x8, Ta[e]), (f32x4{0.f, 0.f, 0.f, 0.f}));
      db[e] = MFMA16(__builtin_bit_cast(bf16x8, mv), __builtin_bit_cast(bf16x8, Tb[e]), (f32x4{0.f, 0.f, 0.f, 0.f}));
    }
    asm volatile("s_nop 7" : "+v"(da[0]), "+v"(da[1]), "+v"(da[2]), "+v"(da[3]), "+v"(db[0]), "+v"(db[1]), "+v"(db[2]), "+v"(db[3]));
    if (lane < 16) {
      const char* ub = ws + OFF_UVP + (kt * 72 + sp) * 1024;
      const unsigned uo = lane * 8;
#pragma unroll
      for (int e = 0; e < 4; ++e) {
        asm volatile("global_store_dwordx2 %0, %1, %2 offset:%3 sc1" ::"v"(uo), "v"(f32x2{da[e][0], da[e][1]}), "s"(ub), "i"(e * 256) : "memory");
        asm volatile("global_store_dwordx2 %0, %1, %2 offset:%3 sc1" ::"v"(uo), "v"(f32x2{db[e][0], db[e][1]}), "s"(ub), "i"(e * 256 + 128) : "memory");
      }
    }
    asm volatile("s_waitcnt vmcnt(0)" ::: "memory");
    if (lane == 0) add_count(ws + OFF_CNT + (384 + sp) * 128);
  } else if (mainp) {
    const char* d0 = ws + OFF_W2T + f0 * 1024; const char* d1 = d0 + 4096;
    ST16W(voff, Ta[0], d0, 0); ST16W(voff, Ta[1], d0, 1024); ST16W(voff, Ta[2], d0, 2048); ST16W(voff, Ta[3], d0, 3072);
    ST16W(voff, Tb[0], d1, 0); ST16W(voff, Tb[1], d1, 1024); ST16W(voff, Tb[2], d1, 2048); ST16W(voff, Tb[3], d1, 3072);
    asm volatile("s_nop 1" ::"v"(Ta[0]), "v"(Ta[1]), "v"(Ta[2]), "v"(Ta[3]), "v"(Tb[0]), "v"(Tb[1]), "v"(Tb[2]), "v"(Tb[3]) : "memory");
  } else {
    char* d = ws + OFF_W2T + f0 * 1024 + (r16 >> 2) * (144 * 9216) + 8192 + g * 256 + (r16 & 3) * 64;
    *(u32x4*)d = Ta[0]; *(u32x4*)(d + 16) = Ta[1]; *(u32x4*)(d + 32) = Ta[2]; *(u32x4*)(d + 48) = Ta[3];
    *(u32x4*)(d + 9216) = Tb[0]; *(u32x4*)(d + 9232) = Tb[1]; *(u32x4*)(d + 9248) = Tb[2]; *(u32x4*)(d + 9264) = Tb[3];
  }
}
DI void side_uv(unsigned sp, unsigned lane, char* ws) {
  wait_count(ws + OFF_CNT + (384 + sp) * 128, 18u);
  __builtin_amdgcn_fence(__ATOMIC_ACQUIRE, "agent");
  const char* pp = ws + OFF_UVP + sp * 1024;
  f32x4 t = {0.f, 0.f, 0.f, 0.f};
#pragma unroll 6
  for (int k = 0; k < 18; ++k) t += *(const f32x4*)(pp + k * 73728 + lane * 16);
  unsigned s0, s1, s2, s3, f, T;
  w1_place(2 * sp, 0, s0, f, T); w1_place(2 * sp, 1, s1, f, T); w1_place(2 * sp + 1, 0, s2, f, T); w1_place(2 * sp + 1, 1, s3, f, T);
  const unsigned sl = lane < 32 ? (lane < 16 ? s0 : s1) : (lane < 48 ? s2 : s3);
  const float u0 = rbf(t[0]), v0 = rbf(t[1]), u1 = rbf(t[2]), v1 = rbf(t[3]);
  char* d = ws + OFF_UV + sl * 256 + (lane & 15) * 32;
  *(u32x4*)d = u32x4{pk2(u0, t[0] - u0), pk2(u0, v0), pk2(t[1] - v0, v0), 0u};
  *(u32x4*)(d + 16) = u32x4{pk2(u1, t[2] - u1), pk2(u1, v1), pk2(t[3] - v1, v1), 0u};
}

extern "C" __global__ __launch_bounds__(768, 3) void k_attn(const u16* __restrict__ x, const u16* __restrict__ wg, const u16* __restrict__ wu, const u16* __restrict__ wd,
                                                char* __restrict__ ws) {
  extern __shared__ __attribute__((aligned(16))) char smem[];
  const int lane = threadIdx.x & 63, w = wave_id(), b = blockIdx.x, c = b >> 5, tb = (b & 7) * 4 + ((b >> 3) & 3);
  if (w >= 8) {
    const int lw = w - 8;
    const unsigned voff = lane * 16, lds0 = (unsigned)(uintptr_t)smem;
    const char* kvg = ws + OFF_KV;
    __builtin_amdgcn_s_setprio(3);
    if (lane < 40) *(volatile LAS u32x4*)((LAS char*)smem + ATT_PROG + lane * 16) = u32x4{0u, 0u, 0u, 0u};
#pragma unroll 1
    for (int p = lw; p < 39; p += 4) {
      glds<4>(kvg + p * 4096, voff, lds0 + p * 4096);
      if (p >> 2 == 1) asm volatile("s_waitcnt vmcnt(4) lgkmcnt(0)\n\ts_barrier" ::: "memory");
      if (p >> 2 == 3) asm volatile("s_waitcnt vmcnt(8)\n\ts_barrier" ::: "memory");
    }
    asm volatile("s_waitcnt vmcnt(0)" ::: "memory");
    if (lane == 0) __hip_atomic_fetch_add((LAS unsigned*)((LAS char*)smem + ATT_PROG + 4), 1u, __ATOMIC_RELAXED, __HIP_MEMORY_SCOPE_WORKGROUP);
#pragma unroll 1
    for (unsigned did = b * 4 + lw; did < 1944; did += 1024) side_unit(did, lane, wg, wu, wd, ws);
    if (b * 4 + lw >= 952) side_uv(b * 4 + lw - 952, lane, ws);
    const char* wsrc = ws + OFF_WOT + (size_t)c * 92160;
#pragma unroll 1
    for (unsigned spins = 0; spins < (1u << 24) && __builtin_amdgcn_readfirstlane(*(volatile LAS unsigned*)((LAS char*)smem + ATT_PROG)) < 8; ++spins)
      __builtin_amdgcn_s_sleep(8);
#pragma unroll 1
    for (int p = lw; p < 22; p += 4) glds<4>(wsrc + p * 4096, voff, lds0 + p * 4096);
    glds<2>(lw == 0 ? wsrc + 90112 : ws + OFF_MODF + 2 * D * 4 + (lw - 1) * 2304, voff, lds0 + (lw == 0 ? 90112 : OP_MOD + (lw - 1) * 2304));
    glds<1>(ws + OFF_MODF + 2 * D * 4 + (lw < 2 ? 1280 : lw == 2 ? 3584 : 5888), voff, lds0 + OP_MOD + (lw < 2 ? 1280 : lw == 2 ? 3584 : 5888));
    { CODE_ADDR(c0, "k_ffn"); CODE_ADDR(c1, "k_end"); touch_code(c0, c1, (((b >> 3) & 3) * 4 + lw) * 64 + lane); }
    asm volatile("s_waitcnt vmcnt(0)" ::: "memory");
    if (lw == 0) {
      asm volatile("s_barrier\n\ts_barrier\n\ts_barrier" ::: "memory");
      char* cnt = ws + OFF_CNT + (352 + tb) * 128;
      if (lane == 0) add_count(cnt);
      wait_count(cnt, 8u);
      asm volatile("s_barrier" ::: "memory");
    }
    return;
  }
  const int r = lane & 31, h = lane >> 5, th = w >> 2, kq = w & 3;
  bf16x8 q1[4]; u32x2 q2[4];
  {
    const char* qp = ws + OFF_Q1 + ((size_t)(4 * c) * S + tb * 64 + th * 32 + r) * 32 + h * 16;
    const char* qp2 = ws + OFF_Q2 + ((size_t)(4 * c) * S + tb * 64 + th * 32 + r) * 4;
#pragma unroll
    for (int i = 0; i < 4; ++i) {
      q1[i] = *(const bf16x8*)(qp + ((kq + i) & 3) * (S * 32));
      const unsigned t = *(const unsigned*)(qp2 + ((kq + i) & 3) * (S * 4));
      q2[i] = u32x2{h ? 0u : t, 0u};
    }
  }
  constexpr int TS = 4 * KV_TILE;
  const LAS char* kp = (const LAS char*)smem + kq * KV_TILE + lane * 16;
  const int vofs = kq * KV_TILE + 1024 + h * 304 + (r < 18 ? r : 18) * 16, k2ofs = kq * KV_TILE + 2240 + r * 8;
  const LAS char* vp = (const LAS char*)smem + (r > 18 ? ATT_ZERO : vofs);
  const LAS char* k2p = (const LAS char*)smem + (h ? ATT_ZERO : k2ofs);
  int vinc = r > 18 ? 0 : TS, kinc = h ? 0 : TS;
  const f32x16 zero16 = {};
  f32x16 sA, sB, o[4];
  u32x4 p0, p1 = {};
  bf16x8 ka, v0, v1 = {};
  bf16x4 kb;
#pragma unroll
  for (int i = 0; i < 3; ++i) { o[i] = zero16; PIN(o[i]); }
  PIN(kp); PIN(vp); PIN(k2p); PIN(vinc); PIN(kinc);
  asm volatile("s_barrier" ::: "memory");
  ka = *(const LAS bf16x8*)(kp - kq * KV_TILE); kb = *(const LAS bf16x4*)((const LAS char*)smem + 2240 + r * 8);
  bool okm = true;
  f32x16 s0[4];
#define Q2(i) __builtin_bit_cast(bf16x4, q2[i])
#pragma unroll
  for (int i = 0; i < 4; ++i) { s0[i] = MFMA32K8(kb, Q2(i), zero16); s0[i] = MFMA32(ka, q1[i], s0[i]); }
  SBAR();
  ka = *(const LAS bf16x8*)kp; kb = *(const LAS bf16x4*)k2p;
#pragma unroll
  for (int i = 0; i < 4; ++i) {
    float m = __builtin_fmaxf(s0[i][0], s0[i][1]);
#pragma unroll
    for (int j = 2; j < 16; j += 2) m = __builtin_fmaxf(__builtin_fmaxf(m, s0[i][j]), s0[i][j + 1]);
    const auto sw = __builtin_amdgcn_permlane32_swap(__float_as_uint(m), __float_as_uint(m), false, false);
    m = __builtin_fmaxf(__uint_as_float(sw[0]), __uint_as_float(sw[1]));
    const unsigned nm = pk2(-64.f - m, 0.f);
    q2[i][1] = h ? nm << 16 : nm;
    okm = okm && (bf_lo(nm) + m >= -80.f);
  }
  const bool shift_ok = __all(okm);
  asm volatile("s_barrier" ::: "memory");
  o[3] = zero16;
  sA = MFMA32K8(kb, Q2(0), zero16); sA = MFMA32(ka, q1[0], sA);
#define EX(x) x = __builtin_amdgcn_exp2f(x)
#define ATT_STEP(C, N, i)                                                                                 \
  {                                                                                                       \
    constexpr int ip = ((i) + 3) & 3, in = ((i) + 1) & 3;                                                 \
    if ((i) == 0) v0 = *(const LAS bf16x8*)(vp);                                                          \
    SBAR();                                                                                               \
    EX(C[0]); EX(C[1]); PIN(C); SBAR();                                                                   \
    o[ip] = MFMA32(v1, __builtin_bit_cast(bf16x8, p1), o[ip]);                                            \
    SBAR();                                                                                               \
    if ((i) == 0) v1 = *(const LAS bf16x8*)(vp + 608);                                                    \
    if ((i) == 1) { kp += adv * TS; vp += __mul24(vinc, adv); k2p += __mul24(kinc, adv); }                                                  \
    EX(C[2]); EX(C[3]); p0[0] = pk2(C[0], C[1]); EX(C[4]); p0[1] = pk2(C[2], C[3]); EX(C[5]);             \
    PIN(p0); PIN(C); SBAR();                                                                              \
    N = MFMA32K8(kb, Q2(in), zero16);                                                                     \
    SBAR();                                                                                               \
    if ((i) == 2) kb = *(const LAS bf16x4*)(k2p);                                                         \
    EX(C[6]); EX(C[7]); p0[2] = pk2(C[4], C[5]); EX(C[8]); p0[3] = pk2(C[6], C[7]); EX(C[9]);             \
    PIN(p0); PIN(C); SBAR();                                                                              \
    N = MFMA32(ka, q1[in], N);                                                                            \
    SBAR();                                                                                               \
    if ((i) == 2) ka = *(const LAS bf16x8*)(kp);                                                          \
    EX(C[10]); EX(C[11]); p1[0] = pk2(C[8], C[9]); EX(C[12]); p1[1] = pk2(C[10], C[11]); EX(C[13]);       \
    PIN(p1); PIN(C); SBAR();                                                                              \
    o[i] = MFMA32(v0, __builtin_bit_cast(bf16x8, p0), o[i]);                                              \
    SBAR();                                                                                               \
    EX(C[14]); EX(C[15]); p1[2] = pk2(C[12], C[13]); p1[3] = pk2(C[14], C[15]);                           \
    PIN(p1); SBAR();                                                                                      \
  }
  __builtin_amdgcn_s_setprio(2);
#pragma unroll 2
  for (int it = 0; it < 16; ++it) {
    const int adv = it == 15 ? -15 : 1;
    ATT_STEP(sA, sB, 0) ATT_STEP(sB, sA, 1) ATT_STEP(sA, sB, 2) ATT_STEP(sB, sA, 3)
    if (__builtin_expect((0x4402 >> it) & 1, 0)) {
      if (it == 1) {
#pragma unroll 1
        for (unsigned spins = 0; spins < (1u << 24) && __builtin_amdgcn_readfirstlane(*(volatile LAS unsigned*)((LAS char*)smem + ATT_PROG + 4)) < 4; ++spins)
          __builtin_amdgcn_s_sleep(2);
        asm volatile("" ::: "memory");
      }
      else if (it == 10) {
        __builtin_amdgcn_s_setprio(1);
        if (lane == 0) __hip_atomic_fetch_add((LAS unsigned*)((LAS char*)smem + ATT_PROG), 1u, __ATOMIC_RELAXED, __HIP_MEMORY_SCOPE_WORKGROUP);
      }
      else __builtin_amdgcn_s_setprio(0);
    }
  }
  o[3] = MFMA32(v1, __builtin_bit_cast(bf16x8, p1), o[3]);
#undef ATT_STEP
  float ms[4];
#pragma unroll
  for (int i = 0; i < 4; ++i) ms[i] = __uint_as_float(h ? q2[i][1] : q2[i][1] << 16);
  {
    const float lsum = (o[0][10] + o[1][10]) + (o[2][10] + o[3][10]);
    if (__builtin_expect(!shift_ok || !__all(lsum <= 0x1p40f), 0)) {
      int lf = threadIdx.x & 63; PIN(lf);
      const int rf = lf & 31, hf = lf >> 5;
#pragma unroll
      for (int i = 0; i < 4; ++i) { o[i] = zero16; ms[i] = INFINITY; q2[i][1] = 0u; }
#pragma unroll 1
      for (int t = 0; t < 16; ++t) {
        const char* kg = ws + OFF_KV + t * TS + kq * KV_TILE;
        const char* vg = kg + 1024 + hf * 304 + (rf < 18 ? rf : 18) * 16;
        const bf16x8 fka = *(const bf16x8*)(kg + lf * 16), fv0 = *(const bf16x8*)vg, fv1 = *(const bf16x8*)(vg + 608);
        const bf16x4 fkb = *(const bf16x4*)(kg + 2240 + rf * 8);
#pragma unroll
        for (int i = 0; i < 4; ++i) {
          sA = MFMA32K8(fkb, Q2(i), zero16); sA = MFMA32(fka, q1[i], sA);
          float mt = -ms[i];
#pragma unroll
          for (int j = 0; j < 16; ++j) mt = fmaxf(mt, sA[j]);
          mt = fmaxf(mt, __shfl_xor(mt, 32));
          const float al = (-ms[i] == mt) ? 1.f : __builtin_amdgcn_exp2f(-ms[i] - mt);
          ms[i] = -mt;
#pragma unroll
          for (int j = 0; j < 16; ++j) { o[i][j] *= al; sA[j] = __builtin_amdgcn_exp2f(sA[j] - mt); }
#pragma unroll
          for (int j = 0; j < 4; ++j) { p0[j] = pk2(sA[2 * j], sA[2 * j + 1]); p1[j] = pk2(sA[8 + 2 * j], sA[9 + 2 * j]); }
          o[i] = MFMA32(fv0, __builtin_bit_cast(bf16x8, p0), o[i]);
          o[i] = MFMA32(fv1, __builtin_bit_cast(bf16x8, p1), o[i]);
          SBAR();
        }
      }
    }
  }
#undef EX
#undef Q2
  const int g = lane >> 4, token = tb * 64 + (w & 3) * 16 + (lane & 15);
  const int d0 = 72 * c + 16 * g, d1 = 72 * c + 64 + 4 * (g & 1);
  const u32x4 xa = *(const u32x4*)(x + token * D + d0), xb = *(const u32x4*)(x + token * D + d0 + 8);
  const u32x2 xc = *(const u32x2*)(x + token * D + d1);
  constexpr int AH = 2048 + 64;
  LAS char* cm = (LAS char*)smem + OP_SCR + th * 12288 + lane * 16;
  LAS char* ca = (LAS char*)smem + OP_SCR + 24576 + th * (4 * AH);
  u32x4 pm[4];
  LAS char *wm[4], *wa[4];
#pragma unroll
  for (int i = 0; i < 4; ++i) {
    const int hd = (kq + i) & 3;
    if (i) { pm[i] = u32x4{pk2(o[i][0], o[i][1]), pk2(o[i][2], o[i][3]), pk2(o[i][4], o[i][5]), pk2(o[i][6], o[i][7])}; wm[i] = cm + hd * 3072; PIN(pm[i]); PIN(wm[i]); }
    o[i][11] = ms[i]; wa[i] = ca + hd * AH + r * 16; PIN(o[i]); PIN(wa[i]);
  }
  const LAS char* re = ca + (lane & 3) * AH + (kq * 8 + (r >> 2)) * 16;
  const LAS char* rm = cm + kq * 3072;
  unsigned oo1 = (r >> 4) * (72 * 256) + (2 * kq + h) * 256 + (r & 15) * 16, oo2 = (kq >> 1) * (72 * 256) + 8 * 256 + (kq & 1) * 128 + r * 4;
  const char* ob = ws + OFF_OT + (size_t)(tb * 4 + th * 2) * (72 * 256) + 9 * c * 256;
  PIN(re); PIN(rm); PIN(oo1); PIN(oo2); asm volatile("" : "+s"(ob));
  LDS_BAR();
#pragma unroll
  for (int i = 1; i < 4; ++i) *(LAS u32x4*)(wm[i] + (i - 1) * 1024) = pm[i];
  if (!h)
#pragma unroll
    for (int i = 0; i < 4; ++i) *(LAS f32x4*)(wa[i] + i * 512) = f32x4{o[i][8], o[i][9], o[i][10], o[i][11]};
  LDS_BAR();
  {
    f32x4 a2[4], e2[4];
    u32x4 am[3];
#pragma unroll
    for (int j = 0; j < 4; ++j) {
      const f32x2 lm = *(const LAS f32x2*)(wa[0] + j * 512 + 8);
      a2[j] = f32x4{0.f, 0.f, lm[0], lm[1]};
      e2[j] = *(const LAS f32x4*)(re + j * 512);
    }
#pragma unroll
    for (int j = 0; j < 3; ++j) am[j] = *(const LAS u32x4*)(rm + j * 1024);
    SBAR();
#define ATT_SCALES(A, sc)                                                                                  \
    {                                                                                                      \
      const float mm = __builtin_fminf(__builtin_fminf(A[0][3], A[1][3]), __builtin_fminf(A[2][3], A[3][3])); \
      _Pragma("unroll") for (int j = 0; j < 4; ++j) sc[j] = (A[j][3] == mm) ? 1.f : __builtin_amdgcn_exp2f(mm - A[j][3]); \
    }
    float oo[8];
#pragma unroll
    for (int m = 0; m < 8; ++m) oo[m] = o[0][m];
    const bool same = a2[0][3] == a2[1][3] && a2[0][3] == a2[2][3] && a2[0][3] == a2[3][3] &&
                      e2[0][3] == e2[1][3] && e2[0][3] == e2[2][3] && e2[0][3] == e2[3][3];
    if (__builtin_expect(!__all(same), 0)) {
      float sc[4], se[4];
      ATT_SCALES(a2, sc) ATT_SCALES(e2, se)
#pragma unroll
      for (int j = 0; j < 4; ++j) { a2[j][2] *= sc[j]; e2[j] *= se[j]; }
#pragma unroll
      for (int m = 0; m < 8; ++m) oo[m] *= sc[0];
#pragma unroll
      for (int j = 0; j < 3; ++j)
#pragma unroll
        for (int m = 0; m < 4; ++m) { oo[2 * m] += bf_lo(am[j][m]) * sc[j + 1]; oo[2 * m + 1] += bf_hi(am[j][m]) * sc[j + 1]; am[j][m] = 0u; }
    }
    const float rl = __builtin_amdgcn_rcpf((a2[0][2] + a2[1][2]) + (a2[2][2] + a2[3][2]));
    const f32x4 es = (e2[0] + e2[1]) + (e2[2] + e2[3]);
    const float rle = __builtin_amdgcn_rcpf(es[2]);
    u32x4 ov;
#pragma unroll
    for (int m = 0; m < 4; ++m)
      ov[m] = pk2(((oo[2 * m] + bf_lo(am[0][m])) + (bf_lo(am[1][m]) + bf_lo(am[2][m]))) * rl,
                  ((oo[2 * m + 1] + bf_hi(am[0][m])) + (bf_hi(am[1][m]) + bf_hi(am[2][m]))) * rl);
    unsigned ev = pk2(es[0] * rle, es[1] * rle);
    PIN(ev);
    ST16W(oo1, ov, ob, 0);
    asm volatile("global_store_dword %0, %1, %2 sc1\n\ts_nop 0" ::"v"(oo2), "v"(ev), "s"(ob), "v"(ov) : "memory");
  }
  asm volatile("s_waitcnt vmcnt(0)\n\ts_barrier" ::: "memory");
  f32x4 acc[5];
  {
    const int tt = w & 3, kh = w >> 2;
    const unsigned vo = lane * 16;
    const char* asrc = ws + OFF_OT + (size_t)(tb * 4 + tt) * (72 * 256) + kh * 9216;
    const char* asrc2 = asrc + 4096; const char* asrc3 = asrc + 8192;
    bf16x8 a[9], wf[OPF][5];
    const LAS char* wl = (const LAS char*)smem + kh * 46080 + vo;
#pragma unroll
    for (int i = 0; i < OPF; ++i)
#pragma unroll
      for (int e = 0; e < 5; ++e) wf[i][e] = *(const LAS bf16x8*)(wl + i * 5120 + e * 1024);
    asm volatile("s_barrier\n\ts_nop 4" : "+s"(asrc), "+s"(asrc2), "+s"(asrc3)::"memory");
#define OP_LD(i) if ((i) < 4) GLD16S(a[(i) % 9], vo, asrc, ((i) & 3) * 1024); else if ((i) < 8) GLD16S(a[(i) % 9], vo, asrc2, ((i) & 3) * 1024); else if ((i) == 8) GLD16S(a[8], vo, asrc3, 0);
    OP_LD(0) OP_LD(1) OP_LD(2) OP_LD(3)
#pragma unroll
    for (int e = 0; e < 5; ++e) acc[e] = f32x4{0.f, 0.f, 0.f, 0.f};
#define OP_STEP(i)                                                                                          \
    {                                                                                                       \
      asm volatile("s_waitcnt vmcnt(%1)" : "+v"(a[i]) : "i"((i) + OPW <= 9 ? OPW - 1 : 8 - (i)) : "memory"); \
      OP_LD((i) + OPW)                                                                                      \
      _Pragma("unroll") for (int e = 0; e < 5; ++e) acc[e] = MFMA16(wf[(i) % OPF][e], a[i], acc[e]);          \
      SBAR();                                                                                               \
      if ((i) + OPF < 9) _Pragma("unroll") for (int e = 0; e < 5; ++e) wf[(i) % OPF][e] = *(const LAS bf16x8*)(wl + ((i) + OPF) * 5120 + e * 1024); \
      SBAR();                                                                                               \
    }
    OP_STEP(0) OP_STEP(1) OP_STEP(2) OP_STEP(3) OP_STEP(4) OP_STEP(5) OP_STEP(6) OP_STEP(7) OP_STEP(8)
    LAS char* rl = (LAS char*)smem + OP_SCR + tt * 5120 + vo;
    if (kh == 1)
#pragma unroll
      for (int e = 0; e < 5; ++e) *(LAS f32x4*)(rl + e * 1024) = acc[e];
    LDS_BAR();
    if (kh == 1) return;
#pragma unroll
    for (int e = 0; e < 5; ++e) acc[e] += *(const LAS f32x4*)(rl + e * 1024);
  }
  const LAS float* ml = (const LAS float*)((const LAS char*)smem + OP_MOD);
  unsigned xo[10], yo[10];
  float s = 0.f, q = 0.f;
#pragma unroll
  for (int i = 0; i < 5; ++i) {
    const f32x4 ga = *(const LAS f32x4*)(ml + (i < 4 ? d0 + 4 * i : d1)), cb = *(const LAS f32x4*)(ml + 2 * D + (i < 4 ? d0 + 4 * i : d1));
    const unsigned w0 = i < 2 ? xa[2 * i] : (i < 4 ? xb[2 * i - 4] : xc[0]), w1 = i < 2 ? xa[2 * i + 1] : (i < 4 ? xb[2 * i - 3] : xc[1]);
    const f32x4 a4 = i < 4 ? f32x4{acc[0][i], acc[1][i], acc[2][i], acc[3][i]} : acc[4];
    const unsigned r0 = pk2(bf_lo(w0) + ga[0] * a4[0], bf_hi(w0) + ga[1] * a4[1]), r1 = pk2(bf_lo(w1) + ga[2] * a4[2], bf_hi(w1) + ga[3] * a4[3]);
    const float v0 = bf_lo(r0), v1 = bf_hi(r0), v2 = bf_lo(r1), v3 = bf_hi(r1);
    xo[2 * i] = r0; xo[2 * i + 1] = r1;
    yo[2 * i] = pk2(v0 * cb[0], v1 * cb[1]); yo[2 * i + 1] = pk2(v2 * cb[2], v3 * cb[3]);
    const float ps = (v0 + v1) + (v2 + v3), pq = v0 * v0 + v1 * v1 + v2 * v2 + v3 * v3;
    if (i < 4 || g < 2) { s += ps; q += pq; }
  }
  const size_t base = (size_t)(token >> 4) * (72 * 256) + (token & 15) * 16 + 9 * c * 256;
  char* x1d = ws + OFF_X1 + base;
  char* yd = ws + OFF_Y + base;
  st16_wt(yd + (2 * g) * 256, u32x4{yo[0], yo[1], yo[2], yo[3]});
  st16_wt(yd + (2 * g + 1) * 256, u32x4{yo[4], yo[5], yo[6], yo[7]});
  st16_wt(x1d + (2 * g) * 256, u32x4{xo[0], xo[1], xo[2], xo[3]});
  st16_wt(x1d + (2 * g + 1) * 256, u32x4{xo[4], xo[5], xo[6], xo[7]});
  if (g < 2) {
    st8_wt(yd + 8 * 256 + g * 8, u32x2{yo[8], yo[9]});
    st8_wt(x1d + 8 * 256 + g * 8, u32x2{xo[8], xo[9]});
  }
  const float sq = swap16_add(swap32_add(s, q), 0.f);
  if (!(g & 1)) *(float*)(ws + OFF_ST + ((size_t)token * 8 + c) * 8 + (g >> 1) * 4) = sq;
}

DI void ln_stats(const f32x4& st, float& mu, float& sd2) {
  float s = st[0] + st[2], q = st[1] + st[3];
  s += __shfl_xor(s, 16); q += __shfl_xor(q, 16);
  s += __shfl_xor(s, 32); q += __shfl_xor(q, 32);
  mu = s * (1.f / D);
  sd2 = fmaxf(q * (1.f / D) - mu * mu, 0.f) + LN_EPS;
}

DI bf16x8 corr_operand(float a, float b, int g) {
  const float ah = rbf(a), bh = rbf(b);
  u32x4 v = {pk2(ah, ah), pk2(a - ah, bh), pk2(bh, b - bh), 0u};
  if (g) v = u32x4{0u, 0u, 0u, 0u};
  return __builtin_bit_cast(bf16x8, v);
}
constexpr int G1_WIN = 5;
constexpr int FF_SLOT = 36864, FF_UV = 4 * FF_SLOT, FF_CR = FF_UV + 5120, FF_LDS = FF_CR + 8 * 544, G2_LOOK = 6;
constexpr int G2_P1 = (16 - G2_LOOK - 2) / 4, G2_P2 = (28 - G2_LOOK - 2) / 4, G2_SG = (G2_LOOK - 2 + 3) / 4;
extern "C" __global__ __launch_bounds__(512, 2) void k_ffn(char* __restrict__ ws, u16* __restrict__ out) {
  extern __shared__ __attribute__((aligned(16))) char smem[];
  const int lane = threadIdx.x & 63, w = wave_id(), b = blockIdx.x, g = lane >> 4;
  const int xg = b & 7, jj = b >> 3, ks = xg >> 1;
  const int rb = (xg & 1) * 4 + (jj & 3), nl = jj >> 2, n = ks * 8 + nl;
  const int tb = (xg & 1) * 8 + (jj >> 2), c = jj & 3, tile = tb * 4 + c;
  const unsigned voff = lane * 16, lds0 = (unsigned)(uintptr_t)smem;
  const char* w1s = ws + OFF_W1T + (size_t)n * (9 * FF_SLOT) + w * 4608;
  const char* w2s = ws + OFF_W2T + ((size_t)c * 144 + ks * 36) * 9216;
#define G1_STAGE(s) glds45((s) < 9 ? w1s + (s) * FF_SLOT : w2s + w * 4608 + ((s) - 9) * FF_SLOT, voff, lds0 + w * 4608 + ((s) & 3) * FF_SLOT);
#define G1_SP(s, j) glds45p<j>((s) < 9 ? w1s + (s) * FF_SLOT : w2s + w * 4608 + ((s) - 9) * FF_SLOT, voff, lds0 + w * 4608 + ((s) & 3) * FF_SLOT);
  const char* ha = ws + OFF_HT + (size_t)(tb * 8 + (w & 3) * 2) * (576 * 256) + ks * 36864;
  const char* hb = ha + 576 * 256;
  bf16x8 h0[12], h1[12];
  unsigned ho = voff;
#define G2_LDH(k) { const unsigned o_ = ho + ((k) >> 2) * 4096; GLD16S(h0[k], o_, ha, ((k) & 3) * 1024); GLD16S(h1[k], o_, hb, ((k) & 3) * 1024); }
  char* cntA = ws + OFF_CNT + 256 * 128;
  f32x4 bc0[9], bc1[9];
  {
    const char* ya = ws + OFF_Y + (size_t)(rb * 16 + w * 2) * (72 * 256);
    const char* yb = ya + 72 * 256;
    bf16x8 y0[18], y1[18];
    f32x4 st0, st1;
    G1_STAGE(0)
    asm volatile("s_nop 4" : "+s"(ya), "+s"(yb));
#define G1_LDY(kt) { const unsigned o_ = voff + ((kt) >> 2) * 4096; GLD16N(y0[kt], o_, ya, ((kt) & 3) * 1024); GLD16N(y1[kt], o_, yb, ((kt) & 3) * 1024); }
#define G1_YA(kt) { const unsigned o_ = voff + ((kt) >> 2) * 4096; GLD16N(y0[kt], o_, ya, ((kt) & 3) * 1024); }
#define G1_YB(kt) { const unsigned o_ = voff + ((kt) >> 2) * 4096; GLD16N(y1[kt], o_, yb, ((kt) & 3) * 1024); }
    asm volatile("s_barrier" ::: "memory");
    G1_LDY(0) G1_LDY(1) G1_LDY(2)
    if (w < 4) __builtin_amdgcn_s_setprio(1);
    const LAS char* wl0 = (const LAS char*)smem + voff;
    const LAS char* wl1 = wl0 + 49152;
    const LAS char* wl2 = wl0 + 98304;
    asm volatile("" : "+v"(wl1), "+v"(wl2));
    f32x4 acc0[8], acc1[8];
    bf16x8 fa[G1_WIN + 1];
    const char* hd = ws + OFF_HT + (size_t)(rb * 16 + w * 2) * (576 * 256) + ks * 36864;
    const LAS char* uvl = (const LAS char*)smem + FF_UV + (lane & 15) * 16;
#define G1_RF(f) (*(const LAS bf16x8*)(((f) % 144 < 48 ? wl0 : ((f) % 144 < 96 ? wl1 : wl2)) + ((f) % 48) * 1024))
#define G1_RU(t) (*(const LAS bf16x8*)(uvl + (t) * 256))
#define G1_RD(u) ((u) < 144 ? G1_RF(u) : (u) < 152 ? G1_RU((u) - 144) : (u) < 260 ? G1_RF((u) - 8) : (u) < 266 ? G1_RU((u) - 252) : (u) < 338 ? G1_RF((u) - 14) : G1_RU((u) - 324))
#define G1_KB(base, T, kt, e0, e1, B0, B1)                                                              \
    _Pragma("unroll") for (int e = (e0); e < (e1); ++e) {                                               \
      const int u_ = (base) + (base) / 18 + (kt) * (T) + e;                                             \
      if ((kt) == 0) AMFMA16Z(acc0[e], fa[u_ % (G1_WIN + 1)], B0); else AMFMA16(acc0[e], fa[u_ % (G1_WIN + 1)], B0); \
      SBAR();                                                                                           \
      if (u_ + G1_WIN < 342) fa[(u_ + G1_WIN) % (G1_WIN + 1)] = G1_RD(u_ + G1_WIN);                     \
      SBAR();                                                                                           \
      if ((kt) == 0) AMFMA16Z(acc1[e], fa[u_ % (G1_WIN + 1)], B1); else AMFMA16(acc1[e], fa[u_ % (G1_WIN + 1)], B1); \
      SBAR();                                                                                           \
    }
#define G1_K(base, T, kt, e0, e1) if ((kt) == 17 && (e0) == 0) G1_LDCR G1_KB(base, T, kt, e0, e1, y0[kt], y1[kt])
#define A0(kt, e0, e1) G1_K(0, 8, kt, e0, e1)
#define A1(kt, e0, e1) G1_K(144, 6, kt, e0, e1)
#define A2(kt, e0, e1) G1_K(252, 4, kt, e0, e1)
    bf16x8 cr0, cr1;
#define G1_LDCR { unsigned l16_ = voff; asm volatile("" : "+v"(l16_));                                  \
      const LAS char* crl = (const LAS char*)smem + FF_CR + w * 544 + (l16_ < 256 ? l16_ : 256);        \
      cr0 = *(const LAS bf16x8*)crl; cr1 = *(const LAS bf16x8*)(crl + 272); SBAR(); }
#define G1_CORR(base, T) G1_KB(base, T, 18, 0, T, cr0, cr1) asm volatile("s_nop 7" ::: "memory"); SBAR();
#define G1_H(A, e) pk2(RELU(A[(e) & ~1][2 * ((e) & 1)]) * A[(e) | 1][2 * ((e) & 1)], RELU(A[(e) & ~1][2 * ((e) & 1) + 1]) * A[(e) | 1][2 * ((e) & 1) + 1])
#define G1_QUAD(e0, p)                                                                                  \
    st16_wts(hd + (p) * 1024, voff, (u32x4{G1_H(acc0, e0), G1_H(acc0, e0 + 1), G1_H(acc0, e0 + 2), G1_H(acc0, e0 + 3)}));      \
    st16_wts(hd + (p) * 1024 + 576 * 256, voff, (u32x4{G1_H(acc1, e0), G1_H(acc1, e0 + 1), G1_H(acc1, e0 + 2), G1_H(acc1, e0 + 3)}));
#define G1_EPI0 { G1_CORR(0, 8) G1_QUAD(0, 2 * nl) G1_QUAD(4, 2 * nl + 1) SBAR(); }
#define G1_EPI1 { G1_CORR(144, 6) G1_QUAD(0, 16 + nl)                                                     \
      const char* ph = hd + (24 + (nl >> 1)) * 1024 + (nl & 1) * 512;                                   \
      unsigned vo_ = voff; asm volatile("" : "+v"(vo_)); vo_ = ((vo_ >> 1) & 256) | (vo_ & 255) | ((vo_ >> 5) & 8); \
      st8_wts(ph, vo_, (u32x2{G1_H(acc0, 4), G1_H(acc0, 5)})); st8_wts(ph + 576 * 256, vo_, (u32x2{G1_H(acc1, 4), G1_H(acc1, 5)})); SBAR(); }
#define G1_BAR(N) asm volatile("s_waitcnt vmcnt(%0)\n\ts_barrier" ::"i"(N) : "memory")
#define G1_YW(kt, N) asm volatile("s_waitcnt vmcnt(%2)" : "+v"(y0[kt]), "+v"(y1[kt]) : "i"(N) : "memory");
#define G1_FIRST _Pragma("unroll") for (int e = 0; e < G1_WIN; ++e) fa[e] = G1_RD(e);
#define G1_LDUV glds<1>(ws + OFF_UV + n * 4608 + (w < 4 ? w : 4) * 1024, voff, lds0 + FF_UV + (w < 4 ? w : 4) * 1024);
#define G1_LDST { const char* sp = ws + OFF_ST + (size_t)(rb * 256 + w * 32) * 64; const unsigned so = (lane & 15) * 64 + g * 16; \
                  GLD16(st0, so, sp, 0); GLD16N(st1, so, sp, 1024); }
#define G1_STATS { float mu, sd2; asm volatile("" : "+v"(st0), "+v"(st1));                               \
      LAS char* crl = (LAS char*)smem + FF_CR + w * 544 + (lane < 16 ? lane : 16) * 16;                 \
      ln_stats(st0, mu, sd2); *(LAS bf16x8*)crl = corr_operand(-mu, __builtin_amdgcn_sqrtf(sd2), g);    \
      ln_stats(st1, mu, sd2); *(LAS bf16x8*)(crl + 272) = corr_operand(-mu, __builtin_amdgcn_sqrtf(sd2), g); SBAR(); }
#define G1_SIG(st) if (threadIdx.x == 448) add_count(cntA + ((rb * 4 + ks) * 3 + (st)) * 128);
    unsigned pl0;
#define G1_PEEK0 { const char* cp = cntA + (((tb >> 1) * 4 + ks) * 3) * 128; const unsigned z = 0; \
      asm volatile("s_nop 4\n\tglobal_load_dword %0, %1, %2 sc1" : "=v"(pl0) : "v"(z), "s"(cp) : "memory"); }
#define G1_POLL0(N) { asm volatile("s_waitcnt vmcnt(%1)" : "+v"(pl0) : "i"(N) : "memory"); \
      if (w == 6 && (unsigned)__builtin_amdgcn_readfirstlane(pl0) < 8u) wait_count(cntA + (((tb >> 1) * 4 + ks) * 3) * 128, 8u); }
#define G1_BZERO _Pragma("unroll") for (int e = 0; e < 9; ++e) { bc0[e] = f32x4{0.f, 0.f, 0.f, 0.f}; bc1[e] = f32x4{0.f, 0.f, 0.f, 0.f}; PIN(bc0[e]); PIN(bc1[e]); } SBAR();
#define G1_HPRE asm volatile("s_nop 4" : "+s"(ha), "+s"(hb)); if (w < 4) { G2_LDH(0) G2_LDH(1) G2_LDH(2) }
    G1_BAR(6); G1_FIRST G1_SP(1, 0) G1_YW(0, 5) A0(0, 0, 1) G1_SP(1, 1) A0(0, 1, 2) G1_SP(1, 2) A0(0, 2, 3) G1_SP(1, 3) A0(0, 3, 4)
    G1_SP(1, 4) A0(0, 4, 5) G1_YA(3) A0(0, 5, 6) G1_YB(3) A0(0, 6, 7) G1_YA(4) A0(0, 7, 8) G1_YB(4) G1_YW(1, 11) A0(1, 0, 1) G1_YA(5) A0(1, 1, 3)
    G1_YB(5) A0(1, 3, 5) G1_YA(6) A0(1, 5, 7) G1_YB(6) A0(1, 7, 8) G1_YW(2, 13) A0(2, 0, 1) G1_LDST A0(2, 1, 3) G1_LDUV A0(2, 3, 6) G1_YA(7)
    A0(2, 6, 8)
    G1_BAR(12); G1_YB(7) G1_YW(3, 11) A0(3, 0, 2) G1_SP(2, 0) A0(3, 2, 4) G1_SP(2, 1) A0(3, 4, 6) G1_SP(2, 2) A0(3, 6, 8) G1_YW(4, 12) A0(4, 0, 1)
    G1_SP(2, 3) A0(4, 1, 3) G1_SP(2, 4) A0(4, 3, 5) G1_YA(8) A0(4, 5, 7) G1_YB(8) A0(4, 7, 8) G1_YW(5, 14) A0(5, 0, 2) G1_YA(9) A0(5, 2, 4) G1_YB(9)
    A0(5, 4, 6) G1_YA(10) A0(5, 6, 8) G1_YB(10) G1_YW(6, 16) A0(6, 0, 2) G1_YA(11) A0(6, 2, 5) G1_YB(11) A0(6, 5, 7) G1_SP(3, 0) A0(6, 7, 8)
    G1_YW(7, 14) A0(7, 0, 1) G1_SP(3, 1) A0(7, 1, 3) G1_SP(3, 2) A0(7, 3, 5) G1_SP(3, 3) A0(7, 5, 8)
    G1_BAR(12); G1_SP(3, 4) G1_YW(8, 11) A0(8, 0, 2) G1_YA(12) A0(8, 2, 4) G1_YB(12) A0(8, 4, 6) G1_YA(13) A0(8, 6, 8) G1_YB(13) G1_YW(9, 13)
    A0(9, 0, 3) G1_YA(14) A0(9, 3, 5) G1_YB(14) A0(9, 5, 7) G1_YA(15) A0(9, 7, 8) G1_STATS G1_YW(10, 14) A0(10, 0, 1) G1_YB(15) A0(10, 1, 3) G1_YA(16)
    A0(10, 3, 6) G1_YB(16) A0(10, 6, 8) G1_YA(17) G1_YW(11, 16) A0(11, 0, 2) G1_YB(17) A0(11, 2, 4) G1_SP(4, 0) A0(11, 4, 6) G1_SP(4, 1) A0(11, 6, 8)
    G1_BAR(14); G1_YW(12, 12) A0(12, 0, 1) G1_SP(4, 2) A0(12, 1, 3) G1_SP(4, 3) A0(12, 3, 5) G1_SP(4, 4) A0(12, 5, 8) G1_YW(13, 13) A0(13, 0, 8)
    G1_YW(14, 11) A0(14, 0, 8) G1_YW(15, 9) A0(15, 0, 8)
    G1_BAR(0); G1_SP(5, 0) G1_YW(16, 8) A0(16, 0, 1) G1_SP(5, 1) A0(16, 1, 3) G1_SP(5, 2) A0(16, 3, 4) G1_SP(5, 3) A0(16, 4, 6) G1_SP(5, 4)
    A0(16, 6, 7) G1_SP(6, 0) A0(16, 7, 8) G1_YW(17, 11) A0(17, 0, 1) G1_SP(6, 1) A0(17, 1, 2) G1_SP(6, 2) A0(17, 2, 4) G1_SP(6, 3) A0(17, 4, 5)
    G1_SP(6, 4) A0(17, 5, 8)
    G1_EPI0 A1(0, 0, 6) A1(1, 0, 6) A1(2, 0, 6)
    G1_BAR(9); A1(3, 0, 1) G1_SP(7, 0) A1(3, 1, 4) G1_SP(7, 1) A1(3, 4, 6) A1(4, 0, 1) G1_SP(7, 2) A1(4, 1, 4) G1_SP(7, 3) A1(4, 4, 6) A1(5, 0, 1)
    G1_SP(7, 4) A1(5, 1, 6) A1(6, 0, 6) A1(7, 0, 6) A1(8, 0, 6)
    G1_BAR(5); G1_SIG(0) A1(9, 0, 1) G1_SP(8, 0) A1(9, 1, 4) G1_SP(8, 1) A1(9, 4, 6) A1(10, 0, 1) G1_SP(8, 2) A1(10, 1, 4) G1_SP(8, 3) A1(10, 4, 6)
    A1(11, 0, 1) G1_SP(8, 4) A1(11, 1, 6) A1(12, 0, 6) A1(13, 0, 6)
    G1_BAR(5); A1(14, 0, 1) G1_SP(9, 0) A1(14, 1, 4) G1_SP(9, 1) A1(14, 4, 6) A1(15, 0, 1) G1_SP(9, 2) A1(15, 1, 4) G1_SP(9, 3) A1(15, 4, 6)
    A1(16, 0, 1) G1_SP(9, 4) A1(16, 1, 6) A1(17, 0, 6)
    G1_EPI1 A2(0, 0, 4) A2(1, 0, 4) A2(2, 0, 4) A2(3, 0, 4) A2(4, 0, 4)
    G1_BAR(9); G1_PEEK0 A2(5, 0, 1) G1_SP(10, 0) A2(5, 1, 4) G1_SP(10, 1) A2(6, 0, 3) G1_SP(10, 2) A2(6, 3, 4) A2(7, 0, 2) G1_SP(10, 3) A2(7, 2, 4)
    A2(8, 0, 1) G1_SP(10, 4) A2(8, 1, 4) A2(9, 0, 4) A2(10, 0, 4) G1_POLL0(5)
    G1_BAR(6); G1_SIG(1) A2(11, 0, 1) G1_SP(11, 0) A2(11, 1, 3) G1_SP(11, 1) A2(11, 3, 4) A2(12, 0, 1) G1_SP(11, 2) A2(12, 1, 3) G1_SP(11, 3)
    A2(12, 3, 4) A2(13, 0, 1) G1_SP(11, 4) A2(13, 1, 4) G1_HPRE A2(14, 0, 4) A2(15, 0, 4) G1_BZERO A2(16, 0, 4) A2(17, 0, 4)
    if (w < 4) { G2_LDH(3) G2_LDH(4) G2_LDH(5) }
    { G1_CORR(252, 4) G1_QUAD(0, 28 + nl) SBAR(); }
  }
  if (w >= 4) {
    const char* wsrc = w2s + (w - 4) * 9216;
    const unsigned ld = lds0 + (w - 4) * 9216;
#pragma unroll 1
    for (int s = 0; s < 8; ++s) {
      if (s == 0) asm volatile("s_waitcnt vmcnt(7)" ::: "memory");
      if (s >= 7) asm volatile("s_waitcnt vmcnt(0)" ::: "memory");
      asm volatile("s_waitcnt vmcnt(9)\n\ts_barrier" ::: "memory");
      if (s == G2_SG) G1_SIG(2)
      if (w == 6 && (s + 1 == G2_P1 || s + 1 == G2_P2)) wait_count(cntA + (((tb >> 1) * 4 + ks) * 3 + (s + 1 == G2_P1 ? 1 : 2)) * 128, 8u);
      if (s < 6) {
        const char* p_ = wsrc + (s + 3) * FF_SLOT; const unsigned l_ = ld + (s & 3) * FF_SLOT;
        glds<4>(p_, voff, l_); glds<4>(p_ + 4096, voff, l_ + 4096); glds<1>(p_ + 8192, voff, l_ + 8192);
      }
    }
  } else {
    bf16x8 fa[9];
    f32x4 (&acc0)[9] = bc0; f32x4 (&acc1)[9] = bc1;
    const LAS char* wl[3];
    wl[0] = (const LAS char*)smem + voff + FF_SLOT;
#pragma unroll
    for (int e = 0; e < 8; ++e) fa[e] = *(const LAS bf16x8*)(wl[0] + e * 1024);
#define G2_STEP(k)                                                                                        \
    {                                                                                                     \
      asm volatile("s_waitcnt vmcnt(%2)" : "+v"(h0[k]), "+v"(h1[k]) : "i"(2 * G2_LOOK - 2) : "memory");   \
      _Pragma("unroll") for (int e = 0; e < 9; ++e) {                                                     \
        AMFMA16(acc0[e], fa[e], h0[k]);                                                                   \
        SBAR();                                                                                           \
        if (e == 0) { fa[8] = *(const LAS bf16x8*)(wl[(k) >> 2] + ((k) & 3) * 9216 + 8192); if ((k) == 11) wl[0] = nx0; } \
        else fa[e - 1] = *(const LAS bf16x8*)(wl[(((k) + 1) % 12) >> 2] + (((k) + 1) & 3) * 9216 + (e - 1) * 1024); \
        SBAR();                                                                                           \
        AMFMA16(acc1[e], fa[e], h1[k]);                                                                   \
        SBAR();                                                                                           \
        if (e == 2) { G2_LDH(((k) + G2_LOOK) % 12) if ((k) + G2_LOOK == 11) ho += 12288; }  \
      }                                                                                                   \
    }
    asm volatile("s_nop 1" ::: "memory"); SBAR();
#pragma unroll 1
    for (int it = 0; it < 2; ++it) {
      wl[1] = (const LAS char*)smem + voff + ((3 * it + 2) & 3) * FF_SLOT;
      wl[2] = (const LAS char*)smem + voff + ((3 * it + 3) & 3) * FF_SLOT;
      const LAS char* nx0 = (const LAS char*)smem + voff + ((3 * it + 4) & 3) * FF_SLOT;
      G2_STEP(0) G2_STEP(1) asm volatile("s_barrier" ::: "memory"); G2_STEP(2) G2_STEP(3)
      G2_STEP(4) G2_STEP(5) asm volatile("s_barrier" ::: "memory"); G2_STEP(6) G2_STEP(7)
      G2_STEP(8) G2_STEP(9) asm volatile("s_barrier" ::: "memory"); G2_STEP(10) G2_STEP(11)
    }
    wl[1] = (const LAS char*)smem + voff; wl[2] = wl[1] + FF_SLOT;
    const char* xd = ws + OFF_XB + ((size_t)(tile * 4 + w) * 4 + ks) * 9216;
    const char* xd1 = xd + 4096; const char* xd2 = xd + 8192;
    LAS char* xl = (LAS char*)smem + 2 * FF_SLOT + voff;
#define G3_K(p) ((p) < 36 ? (p) / 9 : ((p) < 76 ? 4 + ((p) - 36) / 5 : 4 + ((p) - 76) / 4))
#define G3_E(p) ((p) < 36 ? (p) % 9 : ((p) < 76 ? 4 + ((p) - 36) % 5 : ((p) - 76) % 4))
#define G3_RUN(p0, p1)                                                                                    \
    _Pragma("unroll") for (int p = (p0); p < (p1); ++p) {                                                 \
      AMFMA16(acc0[G3_E(p)], fa[p % 9], h0[G3_K(p)]);                                                     \
      SBAR();                                                                                             \
      if (p + 8 < 108) fa[(p + 8) % 9] = *(const LAS bf16x8*)(wl[G3_K(p + 8) >> 2] + (G3_K(p + 8) & 3) * 9216 + G3_E(p + 8) * 1024); \
      SBAR();                                                                                             \
      AMFMA16(acc1[G3_E(p)], fa[p % 9], h1[G3_K(p)]);                                                     \
      SBAR();                                                                                             \
    }
#define G3_W(k, N) asm volatile("s_waitcnt vmcnt(%2)" : "+v"(h0[k]), "+v"(h1[k]) : "i"(N) : "memory");
#define G2_PK(A, q, i) pk2(A[4 * (q)][i], A[4 * (q) + 1][i]), pk2(A[4 * (q) + 2][i], A[4 * (q) + 3][i])
    u32x4 pk[9];
#define G3_OUT(q)                                                                                         \
    if (w == ks) {                                                                                        \
      _Pragma("unroll") for (int e = 4 * (q); e < 4 + 5 * (q); ++e) { *(LAS f32x4*)(xl + e * 1024) = acc0[e]; *(LAS f32x4*)(xl + 9216 + e * 1024) = acc1[e]; } \
    } else {                                                                                              \
      pk[2 * (q)] = u32x4{G2_PK(acc0, q, 0), G2_PK(acc0, q, 1)}; ST16W(voff, pk[2 * (q)], xd, (q) * 2048); \
      pk[2 * (q) + 1] = u32x4{G2_PK(acc0, q, 2), G2_PK(acc0, q, 3)}; ST16W(voff, pk[2 * (q) + 1], xd, (q) * 2048 + 1024); \
      pk[4 + 2 * (q)] = u32x4{G2_PK(acc1, q, 0), G2_PK(acc1, q, 1)}; ST16W(voff, pk[4 + 2 * (q)], xd1, (q) * 2048); \
      pk[5 + 2 * (q)] = u32x4{G2_PK(acc1, q, 2), G2_PK(acc1, q, 3)}; ST16W(voff, pk[5 + 2 * (q)], xd1, (q) * 2048 + 1024); \
      if (q) { pk[8] = u32x4{pk2(acc0[8][0], acc0[8][1]), pk2(acc0[8][2], acc0[8][3]), pk2(acc1[8][0], acc1[8][1]), pk2(acc1[8][2], acc1[8][3])}; \
               ST16W(voff, pk[8], xd2, 0); asm volatile("" ::"v"(pk[8])); }                               \
      asm volatile("s_nop 1" ::"v"(pk[2 * (q)]), "v"(pk[2 * (q) + 1]), "v"(pk[4 + 2 * (q)]), "v"(pk[5 + 2 * (q)]));     \
    }                                                                                                     \
    SBAR();
    G3_W(0, 10) G3_RUN(0, 3) G2_LDH(6) G3_RUN(3, 7) G2_LDH(7) G3_RUN(7, 9)
    G3_W(1, 12) G3_RUN(9, 12) G2_LDH(8) G3_RUN(12, 16) G2_LDH(9) G3_RUN(16, 18)
    asm volatile("s_barrier" : "+s"(xd), "+s"(xd1), "+s"(xd2)::"memory");
    G3_W(2, 14) G3_RUN(18, 21) G2_LDH(10) G3_RUN(21, 25) G2_LDH(11) G3_RUN(25, 27)
    G3_W(3, 16) G3_RUN(27, 36)
    G3_W(4, 14) G3_RUN(36, 41) G3_W(5, 12) G3_RUN(41, 46)
    asm volatile("s_barrier" ::: "memory");
    G3_W(6, 10) G3_RUN(46, 51) G3_W(7, 8) G3_RUN(51, 56) G3_W(8, 6) G3_RUN(56, 61) G3_W(9, 4) G3_RUN(61, 66)
    G3_W(10, 2) G3_RUN(66, 71) G3_W(11, 0) G3_RUN(71, 79)
    G3_OUT(1)
    G3_RUN(79, 108)
    asm volatile("s_waitcnt vmcnt(0)\n\ts_nop 7" ::: "memory");
    if (w != ks && lane == 0) add_count(ws + OFF_CNT + (512 + tile * 4 + w) * 128);
    SBAR();
    G3_OUT(0)
    asm volatile("s_waitcnt vmcnt(0) lgkmcnt(0)" ::: "memory");
    if (w == ks) { wait_count_fast(ws + OFF_CNT + (512 + tile * 4 + ks) * 128, 3u); wait_count_fast(ws + OFF_CNT + (tile * 4 + ks) * 128, 3u); }
    else {
      if (lane == 0) add_count(ws + OFF_CNT + (tile * 4 + w) * 128);
      asm volatile("s_barrier\n\ts_barrier" ::: "memory");
    }
    return;
  }
  {
    const int tt = w & 1, hm = (w >> 1) & 1;
    const int token = tb * 128 + ks * 32 + tt * 16 + (lane & 15);
    const char* x1p = ws + OFF_X1 + (size_t)(token >> 4) * (72 * 256) + (token & 15) * 16 + 18 * c * 256;
    const float* gbp = (const float*)(ws + OFF_MODF) + 5 * D + 144 * c;
    const u32x4 xa = *(const u32x4*)(x1p + (2 * g + hm) * 256), xb = *(const u32x4*)(x1p + (8 + 2 * g + hm) * 256);
    const u32x2 xc = *(const u32x2*)(x1p + (16 + (g >> 1)) * 256 + (g & 1) * 8);
    f32x4 gb[5];
#pragma unroll
    for (int i = 0; i < 4; ++i) gb[i] = *(const f32x4*)(gbp + 64 * (i >> 1) + 16 * g + 8 * hm + 4 * (i & 1));
    gb[4] = *(const f32x4*)(gbp + 128 + 4 * g);
    float rv;
    { float mu, sd2; ln_stats(*(const f32x4*)(ws + OFF_ST + (size_t)token * 64 + g * 16), mu, sd2); rv = __builtin_amdgcn_rcpf(sd2); }
    float base[20];
#pragma unroll
    for (int i = 0; i < 5; ++i) {
      gb[i] *= rv; PIN(gb[i]);
      const unsigned w0 = i < 2 ? xa[2 * i] : (i < 4 ? xb[2 * i - 4] : xc[0]), w1 = i < 2 ? xa[2 * i + 1] : (i < 4 ? xb[2 * i - 3] : xc[1]);
      base[4 * i] = bf_lo(w0); base[4 * i + 1] = bf_hi(w0); base[4 * i + 2] = bf_lo(w1); base[4 * i + 3] = bf_hi(w1);
    }
    const unsigned fo = voff + tt * 4096 + hm * 1024, fe = voff + 8192;
    const char* xs = ws + OFF_XB + (size_t)(tile * 4 + ks) * (4 * 9216);
    const char* p1 = xs + ((ks + 1) & 3) * 9216; const char* p2 = xs + ((ks + 2) & 3) * 9216; const char* p3 = xs + ((ks + 3) & 3) * 9216;
    const LAS char* lp = (const LAS char*)smem + 2 * FF_SLOT + voff + tt * 9216;
    u16* op = out + (size_t)token * D + 144 * c;
    u32x4 a[3], bq[3], cq[3];
    asm volatile("s_barrier\n\ts_nop 4" : "+s"(p1), "+s"(p2), "+s"(p3)::"memory");
    GLD16S(a[1], fo, p1, 2048); GLD16S(bq[1], fo, p2, 2048); GLD16S(cq[1], fo, p3, 2048);
    GLD16S(a[2], fe, p1, 0); GLD16S(bq[2], fe, p2, 0); GLD16S(cq[2], fe, p3, 0);
    f32x2 r[8];
#pragma unroll
    for (int e = 0; e < 8; ++e) r[e] = *(const LAS f32x2*)(lp + e * 1024 + hm * 8);
    const f32x4 r8 = *(const LAS f32x4*)(lp + 8192);
#pragma unroll
    for (int i = 0; i < 5; ++i)
#pragma unroll
      for (int k = 0; k < 4; ++k) { base[4 * i + k] += gb[i][k] * (i < 4 ? r[4 * (i >> 1) + k][i & 1] : r8[k]); PIN(base[4 * i + k]); }
    asm volatile("s_barrier" ::: "memory");
    GLD16S(a[0], fo, p1, 0); GLD16S(bq[0], fo, p2, 0); GLD16S(cq[0], fo, p3, 0);
#define G2_SUM(F, a, b, c) ((F(a) + F(b)) + F(c))
#define G2_OUT(m, ua, ub, uc) pk2(base[2 * (m)] + gb[(m) >> 1][2 * ((m) & 1)] * G2_SUM(bf_lo, ua, ub, uc), base[2 * (m) + 1] + gb[(m) >> 1][2 * ((m) & 1) + 1] * G2_SUM(bf_hi, ua, ub, uc))
    asm volatile("s_waitcnt vmcnt(3)" : "+v"(a[1]), "+v"(a[2]), "+v"(bq[1]), "+v"(bq[2]), "+v"(cq[1]), "+v"(cq[2])::"memory");
    u32x4 o0, o1;
#pragma unroll
    for (int m = 0; m < 4; ++m) o1[m] = G2_OUT(4 + m, a[1][m], bq[1][m], cq[1][m]);
    const unsigned a0 = tt ? a[2][2] : a[2][0], a1 = tt ? a[2][3] : a[2][1], b0 = tt ? bq[2][2] : bq[2][0], b1 = tt ? bq[2][3] : bq[2][1],
                   c0 = tt ? cq[2][2] : cq[2][0], c1 = tt ? cq[2][3] : cq[2][1];
    u32x2 o2 = {G2_OUT(8, a0, b0, c0), G2_OUT(9, a1, b1, c1)};
    asm volatile("s_waitcnt vmcnt(0)" : "+v"(a[0]), "+v"(bq[0]), "+v"(cq[0]), "+v"(o1), "+v"(o2)::"memory");
    *(u32x4*)(op + 64 + 16 * g + 8 * hm) = o1;
    if (hm == 0) *(u32x2*)(op + 128 + 4 * g) = o2;
#pragma unroll
    for (int m = 0; m < 4; ++m) o0[m] = G2_OUT(m, a[0][m], bq[0][m], cq[0][m]);
    *(u32x4*)(op + 16 * g + 8 * hm) = o0;
  }
}

extern "C" __global__ void k_end() {}

extern "C" void kernel_launch(void* const* d_in, const int* in_sizes, int n_in, void* d_out, int out_size, void* d_ws, size_t ws_size,
                              hipStream_t stream) {
  const u16* x = (const u16*)d_in[0];
  const u16* wq = (const u16*)d_in[1];
  const u16* wk = (const u16*)d_in[2];
  const u16* wv = (const u16*)d_in[3];
  const u16* wo = (const u16*)d_in[4];
  const u16* wg = (const u16*)d_in[6];
  const u16* wu = (const u16*)d_in[7];
  const u16* wd = (const u16*)d_in[8];
  const u16* cv = (const u16*)d_in[9];
  const u16* wm = (const u16*)d_in[10];
  char* ws = (char*)d_ws;
  (void)hipFuncSetAttribute((const void*)k_qkv, hipFuncAttributeMaxDynamicSharedMemorySize, SG_LDS);
  (void)hipFuncSetAttribute((const void*)k_attn, hipFuncAttributeMaxDynamicSharedMemorySize, ATT_LDS);
  (void)hipFuncSetAttribute((const void*)k_ffn, hipFuncAttributeMaxDynamicSharedMemorySize, FF_LDS);
  k_prep<<<256, 640, 0, stream>>>(x, wq, wk, wv, wo, cv, wm, ws);
  k_qkv<<<256, 512, SG_LDS, stream>>>(ws);
  k_attn<<<256, 768, ATT_LDS, stream>>>(x, wg, wu, wd, ws);
  k_ffn<<<256, 512, FF_LDS, stream>>>(ws, (u16*)d_out);
}
```
